```python
import torch

def _softmax(x, axis=-1):
    return torch.softmax(x, dim=axis)
def _gelu(x):
    return torch.nn.functional.gelu(x, approximate="tanh")
def _silu(x):
    return torch.nn.functional.silu(x)
def _sigmoid(x):
    return torch.sigmoid(x)
def _layernorm(x, axis=-1, eps=1e-5):
    m = x.mean(dim=axis, keepdim=True)
    v = x.var(dim=axis, keepdim=True, unbiased=False)
    return (x - m) / torch.sqrt(v + eps)
def _scatter_add(tgt, idx, src, axis=0):
    return tgt.clone().index_add_(axis, idx, src)
def _segment_sum(src, seg, n):
    out = torch.zeros((n,) + src.shape[1:], dtype=src.dtype)
    return out.index_add_(0, seg, src)
def _pad(x, pw):
    flat = tuple(v for pair in reversed(pw) for v in pair)
    return torch.nn.functional.pad(x, flat)

kernel_name = 'standalone__paged_block_table_kv_pagedattention__decode'

def setup_inputs():

    _w = (lambda a: (lambda t: t.to(torch.bfloat16) if t.is_floating_point() else t)(torch.as_tensor(a)))
    B, Hq, Hkv, Sq, Skv, D = 1, 32, 8, 1, 65536, 128
    bs = 16
    nbps = max(1, Skv // bs)
    num_phys = B * nbps + 4
    bt = torch.randperm(num_phys)[: B * nbps].reshape(B, nbps).to(torch.int64)
    cl = (nbps * bs - (torch.arange(1, B + 1, dtype=torch.int64) * 2654435761) % max(1, nbps * bs // 2)).to(torch.int64)
    return {
        "q": _w((lambda *s: torch.randn(*s, dtype=torch.bfloat16) * 0.8)(B, Hq, Sq, D)),
        "k_cache": _w((lambda *s: torch.randn(*s, dtype=torch.bfloat16) * 0.8)(num_phys, bs, Hkv, D)),
        "v_cache": _w((lambda *s: torch.randn(*s, dtype=torch.bfloat16) * 0.05)(num_phys, bs, Hkv, D)),
        "block_table": _w(bt),
        "context_lens": _w(cl),
    }

def reference(q, k_cache, v_cache, block_table, context_lens):
    B, Hq, Sq, D = q.shape
    bs = k_cache.shape[1]
    Hkv = k_cache.shape[2]
    nbps = block_table.shape[1]
    Skv = nbps * bs
    bt = block_table.reshape(-1)
    k = (lambda x, *d: x.permute(*d))(k_cache[bt].reshape(B, Skv, Hkv, D), 0, 2, 1, 3)
    v = (lambda x, *d: x.permute(*d))(v_cache[bt].reshape(B, Skv, Hkv, D), 0, 2, 1, 3)
    g = Hq // Hkv
    qg = q.reshape(B, Hkv, g, Sq, D)
    scores = torch.einsum("bhgqd,bhkd->bhgqk", qg, k) * (D ** -0.5)
    pos = torch.arange(Skv)
    msk = pos[None, :] < context_lens[:, None]
    scores = torch.where(msk[:, None, None, None, :], scores, -1e9)
    p = _softmax(scores, axis=-1)
    out = torch.einsum("bhgqk,bhkd->bhgqd", p, v)
    return out.reshape(B, Hq, Sq, D)
```

```cpp
#include <hip/hip_runtime.h>
#include <hip/hip_bf16.h>
#include <stdint.h>
#define SLEEP 2

namespace {
constexpr int D = 128, WAVES = 8, HPW = 4, NG = 2, NSPLIT = 128, GRID = 256;
constexpr int REC = D + 4;
constexpr unsigned WS_ML = 4096, WS_PAY = WS_ML + 32 * NSPLIT * 16;
#define STAMP(i)

using u32x4 = __attribute__((ext_vector_type(4))) unsigned;
using f32x4 = __attribute__((ext_vector_type(4))) float;
using f32x2 = __attribute__((ext_vector_type(2))) float;
using bf16x2 = __attribute__((ext_vector_type(2))) __bf16;

template <int CTRL> __device__ __forceinline__ float dpp(float x) {
  return __builtin_bit_cast(float, __builtin_amdgcn_mov_dpp(__builtin_bit_cast(int, x), CTRL, 0xf, 0xf, true));
}
__device__ __forceinline__ float xr_max(float x) {
  auto s = __builtin_amdgcn_permlane16_swap(__float_as_uint(x), __float_as_uint(x), false, false);
  x = fmaxf(__uint_as_float(s[0]), __uint_as_float(s[1]));
  auto t = __builtin_amdgcn_permlane32_swap(__float_as_uint(x), __float_as_uint(x), false, false);
  return fmaxf(__uint_as_float(t[0]), __uint_as_float(t[1]));
}
__device__ __forceinline__ float xr_sum(float x) {
  auto s = __builtin_amdgcn_permlane16_swap(__float_as_uint(x), __float_as_uint(x), false, false);
  x = __uint_as_float(s[0]) + __uint_as_float(s[1]);
  auto t = __builtin_amdgcn_permlane32_swap(__float_as_uint(x), __float_as_uint(x), false, false);
  return __uint_as_float(t[0]) + __uint_as_float(t[1]);
}
constexpr int ROR4 = 0x124, ROR8 = 0x128;

__global__ void __launch_bounds__(WAVES * 64, 2)
attn_fused(const unsigned char* __restrict__ q, const unsigned char* __restrict__ kc, const unsigned char* __restrict__ vc,
           const int* __restrict__ bt, const int* __restrict__ lens, unsigned* __restrict__ out, unsigned char* __restrict__ ws) {
  __shared__ __attribute__((aligned(16))) float lds[WAVES * 4 * REC + WAVES * 20];
  STAMP(0);
  const int tid = threadIdx.x, lane = tid & 63, wave = __builtin_amdgcn_readfirstlane(tid >> 6);
  const int r = lane >> 4, c = lane & 15;
  const int g = blockIdx.x % NG, s = blockIdx.x / NG;
  const int head = g * HPW + (wave & 3), p0 = s * 2 + (wave >> 2);
  const int bto = (p0 + 256 * (lane >> 2)) * 4;
  const int qo = ((head * 4 + (c >> 2)) * D + (c & 3) * 32 + r * 8) * 2;
  const int ko = (c >> 2) * 2048 + (c & 3) * 64 + r * 16 + head * 256;
  const int vo = r * 2048 + c * 16 + head * 256;
  const uint64_t ka = (uint64_t)kc, va = (uint64_t)vc;
  unsigned char* ep = ws + g * 256;
  float o0, o1, o2, o3, o4, o5, o6, o7, nmc, l;
  unsigned epoch;
  asm volatile(
      "v_mov_b32 v31, 0\n"
      "global_load_dword v27, %[bto], %[bt]\n"
      "s_load_dword s66, %[lens], 0x0\n"
      "global_load_dwordx4 v[20:23], %[qo], %[qp]\n"
      "global_load_dword v31, v31, %[ep] sc1\n"
      "s_mov_b32 s40, %[klo]\n"
      "s_mov_b32 s41, %[khi]\n"
      "s_mov_b32 s43, 0x20000\n"
      "s_mov_b32 s44, %[vlo]\n"
      "s_mov_b32 s45, %[vhi]\n"
      "s_mov_b32 s47, 0x20000\n"
      "s_mov_b32 s50, 0x8020000\n"
      "s_mov_b32 s61, 0x05040100\n"
      "s_mov_b32 s62, 0x07060302\n"
      "s_mov_b32 s63, 0x3e0293ee\n"
      "s_waitcnt lgkmcnt(0)\n"
      "s_cmp_lt_i32 s66, 1\n"
      "s_cselect_b32 s67, 1, 0\n"
      "s_cselect_b32 s66, 0x10000, s66\n"
      "s_min_i32 s66, s66, 0x10000\n"
      "s_lshr_b32 s68, s66, 12\n"
      "s_and_b32 s69, s66, 0xfff\n"
      "s_add_u32 s70, s69, 3\n"
      "s_lshr_b32 s70, s70, 2\n"
      "s_mul_i32 s71, %[p0], s70\n"
      "s_add_u32 s72, s71, s70\n"
      "s_lshr_b32 s71, s71, 8\n"
      "s_lshr_b32 s72, s72, 8\n"
      "s_lshr_b32 s73, s71, 2\n"
      "s_lshl_b32 s74, s68, 8\n"
      "s_add_u32 s74, s74, s73\n"
      "s_min_u32 s75, s74, 0xfff\n"
      "s_lshl_b32 s75, s75, 2\n"
      "s_load_dword s64, %[bt], s75\n"
      "s_add_u32 s74, s74, 1\n"
      "s_min_u32 s74, s74, 0xfff\n"
      "s_lshl_b32 s74, s74, 2\n"
      "s_load_dword s65, %[bt], s74\n"
      "s_cmp_lt_u32 s71, s72\n"
      "s_addc_u32 s60, s68, 0\n"
      "v_cmp_gt_u32 vcc, s68, %[jl]\n"
      "s_cmp_lg_u32 s60, 0\n"
      "s_cselect_b32 s49, s50, 0\n"
      "s_waitcnt vmcnt(2)\n"
      "v_lshl_add_u32 v27, v27, 15, %[iu]\n"
      "v_cndmask_b32 v27, 1, v27, vcc\n"
      "s_cmp_lg_u32 s68, 0\n"
      "s_cbranch_scc1 2f\n"
      "1:\n"
      "s_waitcnt lgkmcnt(0)\n"
      "s_cmp_lt_u32 s71, s72\n"
      "s_cbranch_scc0 7f\n"
      "s_lshl_b32 s74, s68, 2\n"
      "s_add_u32 s75, s71, 0\n"
      "s_lshr_b32 s76, s75, 2\n"
      "s_cmp_eq_u32 s76, s73\n"
      "s_cselect_b32 s76, s64, s65\n"
      "s_lshl_b32 s76, s76, 15\n"
      "s_and_b32 s77, s75, 3\n"
      "s_lshl_b32 s77, s77, 13\n"
      "s_or_b32 s76, s76, s77\n"
      "s_lshl_b32 s77, s75, 2\n"
      "s_sub_u32 s77, s69, s77\n"
      "s_cmp_lt_u32 s75, s72\n"
      "s_cselect_b32 s76, s76, 1\n"
      "s_cselect_b32 s56, s77, 0\n"
      "s_add_u32 m0, s74, 0\n"
      "s_nop 0\n"
      "v_writelane_b32 v27, s76, m0\n"
      "s_add_u32 s75, s71, 1\n"
      "s_lshr_b32 s76, s75, 2\n"
      "s_cmp_eq_u32 s76, s73\n"
      "s_cselect_b32 s76, s64, s65\n"
      "s_lshl_b32 s76, s76, 15\n"
      "s_and_b32 s77, s75, 3\n"
      "s_lshl_b32 s77, s77, 13\n"
      "s_or_b32 s76, s76, s77\n"
      "s_lshl_b32 s77, s75, 2\n"
      "s_sub_u32 s77, s69, s77\n"
      "s_cmp_lt_u32 s75, s72\n"
      "s_cselect_b32 s76, s76, 1\n"
      "s_cselect_b32 s57, s77, 0\n"
      "s_add_u32 m0, s74, 1\n"
      "s_nop 0\n"
      "v_writelane_b32 v27, s76, m0\n"
      "s_add_u32 s75, s71, 2\n"
      "s_lshr_b32 s76, s75, 2\n"
      "s_cmp_eq_u32 s76, s73\n"
      "s_cselect_b32 s76, s64, s65\n"
      "s_lshl_b32 s76, s76, 15\n"
      "s_and_b32 s77, s75, 3\n"
      "s_lshl_b32 s77, s77, 13\n"
      "s_or_b32 s76, s76, s77\n"
      "s_lshl_b32 s77, s75, 2\n"
      "s_sub_u32 s77, s69, s77\n"
      "s_cmp_lt_u32 s75, s72\n"
      "s_cselect_b32 s76, s76, 1\n"
      "s_cselect_b32 s58, s77, 0\n"
      "s_add_u32 m0, s74, 2\n"
      "s_nop 0\n"
      "v_writelane_b32 v27, s76, m0\n"
      "s_add_u32 s75, s71, 3\n"
      "s_lshr_b32 s76, s75, 2\n"
      "s_cmp_eq_u32 s76, s73\n"
      "s_cselect_b32 s76, s64, s65\n"
      "s_lshl_b32 s76, s76, 15\n"
      "s_and_b32 s77, s75, 3\n"
      "s_lshl_b32 s77, s77, 13\n"
      "s_or_b32 s76, s76, s77\n"
      "s_lshl_b32 s77, s75, 2\n"
      "s_sub_u32 s77, s69, s77\n"
      "s_cmp_lt_u32 s75, s72\n"
      "s_cselect_b32 s76, s76, 1\n"
      "s_cselect_b32 s59, s77, 0\n"
      "s_add_u32 m0, s74, 3\n"
      "s_nop 0\n"
      "v_writelane_b32 v27, s76, m0\n"
      "7:\n"
      "s_cmp_lg_u32 s68, 0\n"
      "s_cbranch_scc1 3f\n"
      "2:\n"
      "s_nop 0\n"
      "v_readlane_b32 s52, v27, 0\n"
      "v_readlane_b32 s53, v27, 1\n"
      "v_readlane_b32 s54, v27, 2\n"
      "v_readlane_b32 s55, v27, 3\n"
      "s_nop 2\n"
      "s_bitcmp1_b32 s52, 0\n"
      "s_cselect_b32 s42, 0, s49\n"
      "buffer_load_dwordx4 v[32:35], %[ko], s[40:43], s52 offen nt\n"
      "s_bitcmp1_b32 s53, 0\n"
      "s_cselect_b32 s42, 0, s49\n"
      "buffer_load_dwordx4 v[36:39], %[ko], s[40:43], s53 offen nt\n"
      "s_bitcmp1_b32 s54, 0\n"
      "s_cselect_b32 s42, 0, s49\n"
      "buffer_load_dwordx4 v[40:43], %[ko], s[40:43], s54 offen nt\n"
      "s_bitcmp1_b32 s55, 0\n"
      "s_cselect_b32 s42, 0, s49\n"
      "buffer_load_dwordx4 v[44:47], %[ko], s[40:43], s55 offen nt\n"
      "s_bitcmp1_b32 s52, 0\n"
      "s_cselect_b32 s46, 0, s49\n"
      "buffer_load_dwordx4 v[48:51], %[vo], s[44:47], s52 offen nt\n"
      "s_bitcmp1_b32 s53, 0\n"
      "s_cselect_b32 s46, 0, s49\n"
      "buffer_load_dwordx4 v[52:55], %[vo], s[44:47], s53 offen nt\n"
      "s_bitcmp1_b32 s54, 0\n"
      "s_cselect_b32 s46, 0, s49\n"
      "buffer_load_dwordx4 v[56:59], %[vo], s[44:47], s54 offen nt\n"
      "s_bitcmp1_b32 s55, 0\n"
      "s_cselect_b32 s46, 0, s49\n"
      "buffer_load_dwordx4 v[60:63], %[vo], s[44:47], s55 offen nt\n"
      "s_cmp_lg_u32 s68, 0\n"
      "s_cbranch_scc1 1b\n"
      "3:\n"
      "v_mov_b32 v96, 0\n"
      "v_mov_b32 v97, 0\n"
      "v_mov_b32 v98, 0\n"
      "v_mov_b32 v99, 0\n"
      "v_mov_b32 v100, 0\n"
      "v_mov_b32 v101, 0\n"
      "v_mov_b32 v102, 0\n"
      "v_mov_b32 v103, 0\n"
      "v_mov_b32 v104, 0\n"
      "v_mov_b32 v105, 0\n"
      "v_mov_b32 v106, 0\n"
      "v_mov_b32 v107, 0\n"
      "v_mov_b32 v108, 0\n"
      "v_mov_b32 v109, 0\n"
      "v_mov_b32 v110, 0\n"
      "v_mov_b32 v111, 0\n"
      "v_mov_b32 v112, 0\n"
      "v_mov_b32 v113, 0\n"
      "v_mov_b32 v114, 0\n"
      "v_mov_b32 v115, 0\n"
      "v_mov_b32 v116, 0\n"
      "v_mov_b32 v117, 0\n"
      "v_mov_b32 v118, 0\n"
      "v_mov_b32 v119, 0\n"
      "v_mov_b32 v120, 0\n"
      "v_mov_b32 v121, 0\n"
      "v_mov_b32 v122, 0\n"
      "v_mov_b32 v123, 0\n"
      "v_mov_b32 v124, 0\n"
      "v_mov_b32 v125, 0\n"
      "v_mov_b32 v126, 0\n"
      "v_mov_b32 v127, 0\n"
      "v_mov_b32 v28, 0xff800000\n"
      "v_mov_b32 v29, 0x7f800000\n"
      "v_mov_b32 v30, 0\n"
      "s_add_u32 s68, s68, 1\n"
      "s_mov_b32 s48, 0\n"
      "s_cmp_eq_u32 s67, 0\n"
      "s_cbranch_scc1 8f\n"
      "s_waitcnt vmcnt(8)\n"
      "v_mov_b32 v20, 0\n"
      "v_mov_b32 v21, 0\n"
      "v_mov_b32 v22, 0\n"
      "v_mov_b32 v23, 0\n"
      "8:\n"
      "s_cmp_eq_u32 s60, 0\n"
      "s_cbranch_scc1 6f\n"
      "4:\n"
      "s_add_u32 s48, s48, 1\n"
      "s_cmp_lt_u32 s48, s60\n"
      "s_cselect_b32 s49, s50, 0\n"
      "s_lshl_b32 s51, s48, 2\n"
      "v_readlane_b32 s52, v27, s51\n"
      "s_add_u32 s51, s51, 1\n"
      "v_readlane_b32 s53, v27, s51\n"
      "s_add_u32 s51, s51, 1\n"
      "v_readlane_b32 s54, v27, s51\n"
      "s_add_u32 s51, s51, 1\n"
      "v_readlane_b32 s55, v27, s51\n"
      "s_waitcnt vmcnt(4)\n"
      "v_mfma_f32_16x16x32_bf16 v[64:67], v[32:35], v[20:23], 0\n"
      "v_mfma_f32_16x16x32_bf16 v[68:71], v[36:39], v[20:23], 0\n"
      "v_mfma_f32_16x16x32_bf16 v[72:75], v[40:43], v[20:23], 0\n"
      "v_mfma_f32_16x16x32_bf16 v[76:79], v[44:47], v[20:23], 0\n"
      "s_bitcmp1_b32 s52, 0\n"
      "s_cselect_b32 s42, 0, s49\n"
      "buffer_load_dwordx4 v[32:35], %[ko], s[40:43], s52 offen nt\n"
      "s_bitcmp1_b32 s53, 0\n"
      "s_cselect_b32 s42, 0, s49\n"
      "buffer_load_dwordx4 v[36:39], %[ko], s[40:43], s53 offen nt\n"
      "s_bitcmp1_b32 s54, 0\n"
      "s_cselect_b32 s42, 0, s49\n"
      "buffer_load_dwordx4 v[40:43], %[ko], s[40:43], s54 offen nt\n"
      "s_bitcmp1_b32 s55, 0\n"
      "s_cselect_b32 s42, 0, s49\n"
      "buffer_load_dwordx4 v[44:47], %[ko], s[40:43], s55 offen nt\n"
      "v_mov_b32_dpp v64, v64 quad_perm:[0,0,0,0] row_mask:0xf bank_mask:0xf\n"
      "v_mov_b32_dpp v68, v68 quad_perm:[0,0,0,0] row_mask:0xf bank_mask:0xf\n"
      "v_mov_b32_dpp v72, v72 quad_perm:[0,0,0,0] row_mask:0xf bank_mask:0xf\n"
      "v_mov_b32_dpp v76, v76 quad_perm:[0,0,0,0] row_mask:0xf bank_mask:0xf\n"
      "v_add_f32_dpp v64, v65, v64 quad_perm:[1,1,1,1] row_mask:0xf bank_mask:0xf\n"
      "v_add_f32_dpp v68, v69, v68 quad_perm:[1,1,1,1] row_mask:0xf bank_mask:0xf\n"
      "v_add_f32_dpp v72, v73, v72 quad_perm:[1,1,1,1] row_mask:0xf bank_mask:0xf\n"
      "v_add_f32_dpp v76, v77, v76 quad_perm:[1,1,1,1] row_mask:0xf bank_mask:0xf\n"
      "v_add_f32_dpp v64, v66, v64 quad_perm:[2,2,2,2] row_mask:0xf bank_mask:0xf\n"
      "v_add_f32_dpp v68, v70, v68 quad_perm:[2,2,2,2] row_mask:0xf bank_mask:0xf\n"
      "v_add_f32_dpp v72, v74, v72 quad_perm:[2,2,2,2] row_mask:0xf bank_mask:0xf\n"
      "v_add_f32_dpp v76, v78, v76 quad_perm:[2,2,2,2] row_mask:0xf bank_mask:0xf\n"
      "v_add_f32_dpp v64, v67, v64 quad_perm:[3,3,3,3] row_mask:0xf bank_mask:0xf\n"
      "v_add_f32_dpp v68, v71, v68 quad_perm:[3,3,3,3] row_mask:0xf bank_mask:0xf\n"
      "v_add_f32_dpp v72, v75, v72 quad_perm:[3,3,3,3] row_mask:0xf bank_mask:0xf\n"
      "v_add_f32_dpp v76, v79, v76 quad_perm:[3,3,3,3] row_mask:0xf bank_mask:0xf\n"
      "s_cmp_eq_u32 s48, s68\n"
      "s_cbranch_scc1 10f\n"
      "11:\n"
      "v_max3_f32 v65, v64, v68, v72\n"
      "v_max_f32 v65, v65, v76\n"
      "v_cmp_gt_f32 vcc, v65, v28\n"
      "s_cmp_lg_u64 vcc, 0\n"
      "s_cbranch_scc1 12f\n"
      "13:\n"
      "v_fma_f32 v65, v64, s63, v29\n"
      "v_fma_f32 v69, v68, s63, v29\n"
      "v_fma_f32 v73, v72, s63, v29\n"
      "v_fma_f32 v77, v76, s63, v29\n"
      "v_exp_f32 v65, v65\n"
      "v_exp_f32 v69, v69\n"
      "v_exp_f32 v73, v73\n"
      "v_exp_f32 v77, v77\n"
      "v_cvt_pk_bf16_f32 v66, v65, v69\n"
      "v_add_f32 v64, v65, v69\n"
      "v_cvt_pk_bf16_f32 v67, v73, v77\n"
      "v_add_f32 v68, v73, v77\n"
      "v_add_f32 v30, v30, v64\n"
      "v_add_f32 v30, v30, v68\n"
      "s_waitcnt vmcnt(4)\n"
      "v_perm_b32 v80, v52, v48, s61\n"
      "v_perm_b32 v81, v60, v56, s61\n"
      "v_perm_b32 v82, v52, v48, s62\n"
      "v_perm_b32 v83, v60, v56, s62\n"
      "v_perm_b32 v84, v53, v49, s61\n"
      "v_perm_b32 v85, v61, v57, s61\n"
      "v_perm_b32 v86, v53, v49, s62\n"
      "v_perm_b32 v87, v61, v57, s62\n"
      "v_perm_b32 v88, v54, v50, s61\n"
      "v_perm_b32 v89, v62, v58, s61\n"
      "v_perm_b32 v90, v54, v50, s62\n"
      "v_perm_b32 v91, v62, v58, s62\n"
      "v_perm_b32 v92, v55, v51, s61\n"
      "v_perm_b32 v93, v63, v59, s61\n"
      "v_perm_b32 v94, v55, v51, s62\n"
      "v_perm_b32 v95, v63, v59, s62\n"
      "s_bitcmp1_b32 s52, 0\n"
      "s_cselect_b32 s46, 0, s49\n"
      "buffer_load_dwordx4 v[48:51], %[vo], s[44:47], s52 offen nt\n"
      "s_bitcmp1_b32 s53, 0\n"
      "s_cselect_b32 s46, 0, s49\n"
      "buffer_load_dwordx4 v[52:55], %[vo], s[44:47], s53 offen nt\n"
      "s_bitcmp1_b32 s54, 0\n"
      "s_cselect_b32 s46, 0, s49\n"
      "buffer_load_dwordx4 v[56:59], %[vo], s[44:47], s54 offen nt\n"
      "s_bitcmp1_b32 s55, 0\n"
      "s_cselect_b32 s46, 0, s49\n"
      "buffer_load_dwordx4 v[60:63], %[vo], s[44:47], s55 offen nt\n"
      "v_mfma_f32_16x16x16_bf16 v[96:99], v[66:67], v[80:81], v[96:99]\n"
      "v_mfma_f32_16x16x16_bf16 v[100:103], v[66:67], v[82:83], v[100:103]\n"
      "v_mfma_f32_16x16x16_bf16 v[104:107], v[66:67], v[84:85], v[104:107]\n"
      "v_mfma_f32_16x16x16_bf16 v[108:111], v[66:67], v[86:87], v[108:111]\n"
      "v_mfma_f32_16x16x16_bf16 v[112:115], v[66:67], v[88:89], v[112:115]\n"
      "v_mfma_f32_16x16x16_bf16 v[116:119], v[66:67], v[90:91], v[116:119]\n"
      "v_mfma_f32_16x16x16_bf16 v[120:123], v[66:67], v[92:93], v[120:123]\n"
      "v_mfma_f32_16x16x16_bf16 v[124:127], v[66:67], v[94:95], v[124:127]\n"
      "s_cmp_lt_u32 s48, s60\n"
      "s_cbranch_scc1 4b\n"
      "s_branch 6f\n"
      "10:\n"
      "v_mov_b32 v69, 0xff800000\n"
      "v_cmp_gt_u32 vcc, s56, %[rr]\n"
      "s_nop 1\n"
      "v_cndmask_b32 v64, v69, v64, vcc\n"
      "v_cmp_gt_u32 vcc, s57, %[rr]\n"
      "s_nop 1\n"
      "v_cndmask_b32 v68, v69, v68, vcc\n"
      "v_cmp_gt_u32 vcc, s58, %[rr]\n"
      "s_nop 1\n"
      "v_cndmask_b32 v72, v69, v72, vcc\n"
      "v_cmp_gt_u32 vcc, s59, %[rr]\n"
      "s_nop 1\n"
      "v_cndmask_b32 v76, v69, v76, vcc\n"
      "s_branch 11b\n"
      "12:\n"
      "v_mov_b32 v69, v65\n"
      "s_nop 1\n"
      "v_permlane16_swap_b32 v65, v69\n"
      "s_nop 1\n"
      "v_max_f32 v65, v65, v69\n"
      "v_mov_b32 v69, v65\n"
      "s_nop 1\n"
      "v_permlane32_swap_b32 v65, v69\n"
      "s_nop 1\n"
      "v_max3_f32 v28, v65, v69, v28\n"
      "v_mul_f32 v65, s63, v28\n"
      "v_add_f32 v69, v65, v29\n"
      "v_exp_f32_e64 v69, -v69\n"
      "v_sub_f32 v29, 0, v65\n"
      "v_mul_f32 v30, v30, v69\n"
      "s_nop 1\n"
      "v_mov_b32_dpp v73, v69 row_newbcast:0 row_mask:0x1 bank_mask:0xf\n"
      "s_nop 1\n"
      "v_mov_b32_dpp v73, v69 row_newbcast:4 row_mask:0x2 bank_mask:0xf\n"
      "s_nop 1\n"
      "v_mov_b32_dpp v73, v69 row_newbcast:8 row_mask:0x4 bank_mask:0xf\n"
      "s_nop 1\n"
      "v_mov_b32_dpp v73, v69 row_newbcast:12 row_mask:0x8 bank_mask:0xf\n"
      "s_nop 1\n"
      "v_mul_f32 v96, v73, v96\n"
      "v_mul_f32 v100, v73, v100\n"
      "v_mul_f32 v104, v73, v104\n"
      "v_mul_f32 v108, v73, v108\n"
      "v_mul_f32 v112, v73, v112\n"
      "v_mul_f32 v116, v73, v116\n"
      "v_mul_f32 v120, v73, v120\n"
      "v_mul_f32 v124, v73, v124\n"
      "s_branch 13b\n"
      "6:\n"
      "s_waitcnt vmcnt(0)\n"
      "s_nop 7\n"
      : "={v96}"(o0), "={v100}"(o1), "={v104}"(o2), "={v108}"(o3), "={v112}"(o4), "={v116}"(o5), "={v120}"(o6), "={v124}"(o7),
        "={v29}"(nmc), "={v30}"(l), "={v31}"(epoch)
      : [bto] "{v16}"(bto), [qo] "{v17}"(qo), [ko] "{v18}"(ko), [vo] "{v19}"(vo), [jl] "{v24}"(lane >> 2), [iu] "{v25}"((lane & 3) * 8192), [rr] "{v26}"(r),
        [bt] "s"(bt), [lens] "s"(lens), [qp] "s"(q), [ep] "s"(ep), [p0] "s"(p0), "s"(out),
        [klo] "s"((unsigned)ka), [khi] "s"((unsigned)(ka >> 32) & 0xffffu), [vlo] "s"((unsigned)va), [vhi] "s"((unsigned)(va >> 32) & 0xffffu)
      :
        "s40", "s41", "s42", "s43", "s44", "s45", "s46", "s47", "s48", "s49", "s50", "s51", "s52", "s53", "s54", "s55", "s56", "s57", "s58", "s59",
        "s60", "s61", "s62", "s63", "s64", "s65", "s66", "s67", "s68", "s69", "s70", "s71", "s72", "s73", "s74", "s75", "s76", "s77", "vcc", "scc",
        "m0", "memory", "v20", "v21", "v22", "v23", "v27", "v28", "v32", "v33", "v34", "v35", "v36", "v37", "v38", "v39", "v40", "v41", "v42", "v43",
        "v44", "v45", "v46", "v47", "v48", "v49", "v50", "v51", "v52", "v53", "v54", "v55", "v56", "v57", "v58", "v59", "v60", "v61", "v62", "v63",
        "v64", "v65", "v66", "v67", "v68", "v69", "v70", "v71", "v72", "v73", "v74", "v75", "v76", "v77", "v78", "v79", "v80", "v81", "v82", "v83",
        "v84", "v85", "v86", "v87", "v88", "v89", "v90", "v91", "v92", "v93", "v94", "v95", "v97", "v98", "v99", "v101", "v102", "v103", "v105",
        "v106", "v107", "v109", "v110", "v111", "v113", "v114", "v115", "v117", "v118", "v119", "v121", "v122", "v123", "v125", "v126", "v127");
  STAMP(1);
  l = xr_sum(l);
  {
    float* rec = lds + (wave * 4 + r) * REC + 4 + c * 8;
    *reinterpret_cast<f32x4*>(rec) = f32x4{o0, o1, o2, o3};
    *reinterpret_cast<f32x4*>(rec + 4) = f32x4{o4, o5, o6, o7};
    if ((lane & 0x33) == 0) *reinterpret_cast<f32x2*>(lds + (wave * 4 + (c >> 2)) * REC) = f32x2{fmaxf(-nmc, -3.0e38f), l};
  }
  const unsigned tag = epoch * 0x9E3779B1u + 0x7F4A7C15u;
  __syncthreads();
  STAMP(2);
  {
    const int hl = tid >> 5, dd = tid & 31;
    const float* ra = lds + hl * REC;
    const float* rb = ra + 16 * REC;
    const f32x2 a = *reinterpret_cast<const f32x2*>(ra), b = *reinterpret_cast<const f32x2*>(rb);
    const f32x4 oa = *reinterpret_cast<const f32x4*>(ra + 4 + dd * 4), ob = *reinterpret_cast<const f32x4*>(rb + 4 + dd * 4);
    const float M = fmaxf(a[0], b[0]);
    const float wa = __builtin_amdgcn_exp2f(a[0] - M), wb = __builtin_amdgcn_exp2f(b[0] - M);
    const f32x4 O = wa * oa + wb * ob;
    u32x4 pay;
#pragma unroll
    for (int i = 0; i < 4; ++i) pay[i] = (__float_as_uint(O[i]) & 0xffffff00u) | ((tag >> (8 * i)) & 0xffu);
    const int H = g * 16 + hl;
    const unsigned po = WS_PAY + ((H * 8 + (dd >> 2)) * NSPLIT + s) * 64 + (dd & 3) * 16;
    asm volatile("global_store_dwordx4 %0, %1, %2 sc1\n s_nop 1" ::"v"(po), "v"(pay), "s"(ws) : "memory");
    if (dd == 0) {
      const u32x4 ml = {__float_as_uint(M), tag, __float_as_uint(wa * a[1] + wb * b[1]), tag};
      const unsigned mo = WS_ML + (H * NSPLIT + s) * 16;
      asm volatile("global_store_dwordx4 %0, %1, %2 sc1\n s_nop 1" ::"v"(mo), "v"(ml), "s"(ws) : "memory");
    }
  }
  STAMP(3);
  const int H = g * 16 + (s >> 3);
  u32x4 pay = {0, 0, 0, 0}, ml = {0, 0, 0, 0};
  {
    const unsigned po = WS_PAY + (H * 8 + (s & 7)) * NSPLIT * 64 + tid * 16, mo = WS_ML + (H * NSPLIT + (tid >> 2)) * 16;
    bool need = true;
#pragma nounroll
    for (int spin = 0; spin < (1 << 17); ++spin) {
      if (need) asm volatile("global_load_dwordx4 %0, %2, %4 sc1\n global_load_dwordx4 %1, %3, %4 sc1\n s_waitcnt vmcnt(0)"
                             : "+v"(pay), "+v"(ml) : "v"(po), "v"(mo), "s"(ws) : "memory");
      const unsigned got = __builtin_amdgcn_perm(__builtin_amdgcn_perm(pay[3], pay[2], 0x0400), __builtin_amdgcn_perm(pay[1], pay[0], 0x0400), 0x05040100);
      unsigned long long b = __ballot(got == tag && ml[1] == tag && ml[3] == tag);
      b &= b >> 1; b &= b >> 2; b &= 0x1111111111111111ull;
      if (b == 0x1111111111111111ull) break;
      need = ((b >> (lane & 60)) & 1) == 0;
      __builtin_amdgcn_s_sleep(SLEEP);
    }
  }
  STAMP(4);
  float* fin = lds + WAVES * 4 * REC;
  {
    const float m = __uint_as_float(ml[0]);
    float M = fmaxf(m, dpp<ROR4>(m)); M = fmaxf(M, dpp<ROR8>(M)); M = xr_max(M);
    const float w = __builtin_amdgcn_exp2f(m - M);
    float v[5] = {w * __uint_as_float(pay[0]), w * __uint_as_float(pay[1]), w * __uint_as_float(pay[2]), w * __uint_as_float(pay[3]), w * __uint_as_float(ml[2])};
#pragma unroll
    for (int i = 0; i < 5; ++i) { v[i] += dpp<ROR4>(v[i]); v[i] += dpp<ROR8>(v[i]); v[i] = xr_sum(v[i]); }
    if (lane < 4) {
      *reinterpret_cast<f32x4*>(fin + wave * 20 + 4 + lane * 4) = f32x4{v[0], v[1], v[2], v[3]};
      if (lane == 0) *reinterpret_cast<f32x2*>(fin + wave * 20) = f32x2{M, v[4]};
    }
  }
  __syncthreads();
  STAMP(5);
  if (wave == 0) {
    const int w8 = lane >> 3, dp = lane & 7;
    const f32x2 a = *reinterpret_cast<const f32x2*>(fin + w8 * 20), o = *reinterpret_cast<const f32x2*>(fin + w8 * 20 + 4 + dp * 2);
    float M = fmaxf(a[0], dpp<ROR8>(a[0])); M = xr_max(M);
    const float w = __builtin_amdgcn_exp2f(a[0] - M);
    float v[3] = {w * o[0], w * o[1], w * a[1]};
#pragma unroll
    for (int i = 0; i < 3; ++i) { v[i] += dpp<ROR8>(v[i]); v[i] = xr_sum(v[i]); }
    const float inv = 1.0f / v[2];
    const bf16x2 res = {(__bf16)(v[0] * inv), (__bf16)(v[1] * inv)};
    if (lane < 8) out[H * 64 + (s & 7) * 8 + lane] = __builtin_bit_cast(unsigned, res);
    if (s == 0 && lane == 0) __hip_atomic_store(reinterpret_cast<unsigned*>(ep), epoch + 1, __ATOMIC_RELAXED, __HIP_MEMORY_SCOPE_AGENT);
  }
}
}

extern "C" void kernel_launch(void* const* d_in, const int* in_sizes, int n_in, void* d_out, int out_size, void* d_ws, size_t ws_size,
                              hipStream_t stream) {
  attn_fused<<<GRID, WAVES * 64, 0, stream>>>((const unsigned char*)d_in[0], (const unsigned char*)d_in[1], (const unsigned char*)d_in[2],
                                              (const int*)d_in[3], (const int*)d_in[4], (unsigned*)d_out, (unsigned char*)d_ws);
}
```

```cpp
#include <hip/hip_runtime.h>
#include <hip/hip_bf16.h>
#include <stdint.h>
#define SLEEP 2
#define PROBE 5
#define PWG 40

namespace {
constexpr int D = 128, WAVES = 8, HPW = 4, NG = 2, NSPLIT = 128, GRID = 256;
constexpr int REC = D + 4;
constexpr unsigned WS_ML = 4096, WS_PAY = WS_ML + 32 * NSPLIT * 16;
#define STAMP(i) ts[i] = __builtin_amdgcn_s_memrealtime()

using u32x4 = __attribute__((ext_vector_type(4))) unsigned;
using f32x4 = __attribute__((ext_vector_type(4))) float;
using f32x2 = __attribute__((ext_vector_type(2))) float;
using bf16x2 = __attribute__((ext_vector_type(2))) __bf16;

template <int CTRL> __device__ __forceinline__ float dpp(float x) {
  return __builtin_bit_cast(float, __builtin_amdgcn_mov_dpp(__builtin_bit_cast(int, x), CTRL, 0xf, 0xf, true));
}
__device__ __forceinline__ float xr_max(float x) {
  auto s = __builtin_amdgcn_permlane16_swap(__float_as_uint(x), __float_as_uint(x), false, false);
  x = fmaxf(__uint_as_float(s[0]), __uint_as_float(s[1]));
  auto t = __builtin_amdgcn_permlane32_swap(__float_as_uint(x), __float_as_uint(x), false, false);
  return fmaxf(__uint_as_float(t[0]), __uint_as_float(t[1]));
}
__device__ __forceinline__ float xr_sum(float x) {
  auto s = __builtin_amdgcn_permlane16_swap(__float_as_uint(x), __float_as_uint(x), false, false);
  x = __uint_as_float(s[0]) + __uint_as_float(s[1]);
  auto t = __builtin_amdgcn_permlane32_swap(__float_as_uint(x), __float_as_uint(x), false, false);
  return __uint_as_float(t[0]) + __uint_as_float(t[1]);
}
constexpr int ROR4 = 0x124, ROR8 = 0x128;

__global__ void __launch_bounds__(WAVES * 64, 2)
attn_fused(const unsigned char* __restrict__ q, const unsigned char* __restrict__ kc, const unsigned char* __restrict__ vc,
           const int* __restrict__ bt, const int* __restrict__ lens, unsigned* __restrict__ out, unsigned char* __restrict__ ws) {
  __shared__ __attribute__((aligned(16))) float lds[WAVES * 4 * REC + WAVES * 20];
  unsigned long long ts[8] = {}, tfirst = 0;
  STAMP(0);
  const int tid = threadIdx.x, lane = tid & 63, wave = __builtin_amdgcn_readfirstlane(tid >> 6);
  const int r = lane >> 4, c = lane & 15;
  const int g = blockIdx.x % NG, s = blockIdx.x / NG;
  const int head = g * HPW + (wave & 3), p0 = s * 2 + (wave >> 2);
  const int bto = (p0 + 256 * (lane >> 2)) * 4;
  const int qo = ((head * 4 + (c >> 2)) * D + (c & 3) * 32 + r * 8) * 2;
  const int ko = (c >> 2) * 2048 + (c & 3) * 64 + r * 16 + head * 256;
  const int vo = r * 2048 + c * 16 + head * 256;
  const uint64_t ka = (uint64_t)kc, va = (uint64_t)vc;
  unsigned char* ep = ws + g * 256;
  float o0, o1, o2, o3, o4, o5, o6, o7, nmc, l;
  unsigned epoch;
  asm volatile(
      "v_mov_b32 v31, 0\n"
      "global_load_dword v27, %[bto], %[bt]\n"
      "s_load_dword s66, %[lens], 0x0\n"
      "global_load_dwordx4 v[20:23], %[qo], %[qp]\n"
      "global_load_dword v31, v31, %[ep] sc1\n"
      "s_mov_b32 s40, %[klo]\n"
      "s_mov_b32 s41, %[khi]\n"
      "s_mov_b32 s43, 0x20000\n"
      "s_mov_b32 s44, %[vlo]\n"
      "s_mov_b32 s45, %[vhi]\n"
      "s_mov_b32 s47, 0x20000\n"
      "s_mov_b32 s50, 0x8020000\n"
      "s_mov_b32 s61, 0x05040100\n"
      "s_mov_b32 s62, 0x07060302\n"
      "s_mov_b32 s63, 0x3e0293ee\n"
      "s_waitcnt lgkmcnt(0)\n"
      "s_cmp_lt_i32 s66, 1\n"
      "s_cselect_b32 s67, 1, 0\n"
      "s_cselect_b32 s66, 0x10000, s66\n"
      "s_min_i32 s66, s66, 0x10000\n"
      "s_lshr_b32 s68, s66, 12\n"
      "s_and_b32 s69, s66, 0xfff\n"
      "s_add_u32 s70, s69, 3\n"
      "s_lshr_b32 s70, s70, 2\n"
      "s_mul_i32 s71, %[p0], s70\n"
      "s_add_u32 s72, s71, s70\n"
      "s_lshr_b32 s71, s71, 8\n"
      "s_lshr_b32 s72, s72, 8\n"
      "s_lshr_b32 s73, s71, 2\n"
      "s_lshl_b32 s74, s68, 8\n"
      "s_add_u32 s74, s74, s73\n"
      "s_min_u32 s75, s74, 0xfff\n"
      "s_lshl_b32 s75, s75, 2\n"
      "s_load_dword s64, %[bt], s75\n"
      "s_add_u32 s74, s74, 1\n"
      "s_min_u32 s74, s74, 0xfff\n"
      "s_lshl_b32 s74, s74, 2\n"
      "s_load_dword s65, %[bt], s74\n"
      "s_cmp_lt_u32 s71, s72\n"
      "s_addc_u32 s60, s68, 0\n"
      "v_cmp_gt_u32 vcc, s68, %[jl]\n"
      "s_cmp_lg_u32 s60, 0\n"
      "s_cselect_b32 s49, s50, 0\n"
      "s_waitcnt vmcnt(2)\n"
      "s_memrealtime s[78:79]\n"
      "v_lshl_add_u32 v27, v27, 15, %[iu]\n"
      "v_cndmask_b32 v27, 1, v27, vcc\n"
      "s_cmp_lg_u32 s68, 0\n"
      "s_cbranch_scc1 2f\n"
      "1:\n"
      "s_waitcnt lgkmcnt(0)\n"
      "s_cmp_lt_u32 s71, s72\n"
      "s_cbranch_scc0 7f\n"
      "s_lshl_b32 s74, s68, 2\n"
      "s_add_u32 s75, s71, 0\n"
      "s_lshr_b32 s76, s75, 2\n"
      "s_cmp_eq_u32 s76, s73\n"
      "s_cselect_b32 s76, s64, s65\n"
      "s_lshl_b32 s76, s76, 15\n"
      "s_and_b32 s77, s75, 3\n"
      "s_lshl_b32 s77, s77, 13\n"
      "s_or_b32 s76, s76, s77\n"
      "s_lshl_b32 s77, s75, 2\n"
      "s_sub_u32 s77, s69, s77\n"
      "s_cmp_lt_u32 s75, s72\n"
      "s_cselect_b32 s76, s76, 1\n"
      "s_cselect_b32 s56, s77, 0\n"
      "s_add_u32 m0, s74, 0\n"
      "s_nop 0\n"
      "v_writelane_b32 v27, s76, m0\n"
      "s_add_u32 s75, s71, 1\n"
      "s_lshr_b32 s76, s75, 2\n"
      "s_cmp_eq_u32 s76, s73\n"
      "s_cselect_b32 s76, s64, s65\n"
      "s_lshl_b32 s76, s76, 15\n"
      "s_and_b32 s77, s75, 3\n"
      "s_lshl_b32 s77, s77, 13\n"
      "s_or_b32 s76, s76, s77\n"
      "s_lshl_b32 s77, s75, 2\n"
      "s_sub_u32 s77, s69, s77\n"
      "s_cmp_lt_u32 s75, s72\n"
      "s_cselect_b32 s76, s76, 1\n"
      "s_cselect_b32 s57, s77, 0\n"
      "s_add_u32 m0, s74, 1\n"
      "s_nop 0\n"
      "v_writelane_b32 v27, s76, m0\n"
      "s_add_u32 s75, s71, 2\n"
      "s_lshr_b32 s76, s75, 2\n"
      "s_cmp_eq_u32 s76, s73\n"
      "s_cselect_b32 s76, s64, s65\n"
      "s_lshl_b32 s76, s76, 15\n"
      "s_and_b32 s77, s75, 3\n"
      "s_lshl_b32 s77, s77, 13\n"
      "s_or_b32 s76, s76, s77\n"
      "s_lshl_b32 s77, s75, 2\n"
      "s_sub_u32 s77, s69, s77\n"
      "s_cmp_lt_u32 s75, s72\n"
      "s_cselect_b32 s76, s76, 1\n"
      "s_cselect_b32 s58, s77, 0\n"
      "s_add_u32 m0, s74, 2\n"
      "s_nop 0\n"
      "v_writelane_b32 v27, s76, m0\n"
      "s_add_u32 s75, s71, 3\n"
      "s_lshr_b32 s76, s75, 2\n"
      "s_cmp_eq_u32 s76, s73\n"
      "s_cselect_b32 s76, s64, s65\n"
      "s_lshl_b32 s76, s76, 15\n"
      "s_and_b32 s77, s75, 3\n"
      "s_lshl_b32 s77, s77, 13\n"
      "s_or_b32 s76, s76, s77\n"
      "s_lshl_b32 s77, s75, 2\n"
      "s_sub_u32 s77, s69, s77\n"
      "s_cmp_lt_u32 s75, s72\n"
      "s_cselect_b32 s76, s76, 1\n"
      "s_cselect_b32 s59, s77, 0\n"
      "s_add_u32 m0, s74, 3\n"
      "s_nop 0\n"
      "v_writelane_b32 v27, s76, m0\n"
      "7:\n"
      "s_cmp_lg_u32 s68, 0\n"
      "s_cbranch_scc1 3f\n"
      "2:\n"
      "s_nop 0\n"
      "v_readlane_b32 s52, v27, 0\n"
      "v_readlane_b32 s53, v27, 1\n"
      "v_readlane_b32 s54, v27, 2\n"
      "v_readlane_b32 s55, v27, 3\n"
      "s_nop 2\n"
      "s_bitcmp1_b32 s52, 0\n"
      "s_cselect_b32 s42, 0, s49\n"
      "buffer_load_dwordx4 v[32:35], %[ko], s[40:43], s52 offen nt\n"
      "s_bitcmp1_b32 s53, 0\n"
      "s_cselect_b32 s42, 0, s49\n"
      "buffer_load_dwordx4 v[36:39], %[ko], s[40:43], s53 offen nt\n"
      "s_bitcmp1_b32 s54, 0\n"
      "s_cselect_b32 s42, 0, s49\n"
      "buffer_load_dwordx4 v[40:43], %[ko], s[40:43], s54 offen nt\n"
      "s_bitcmp1_b32 s55, 0\n"
      "s_cselect_b32 s42, 0, s49\n"
      "buffer_load_dwordx4 v[44:47], %[ko], s[40:43], s55 offen nt\n"
      "s_bitcmp1_b32 s52, 0\n"
      "s_cselect_b32 s46, 0, s49\n"
      "buffer_load_dwordx4 v[48:51], %[vo], s[44:47], s52 offen nt\n"
      "s_bitcmp1_b32 s53, 0\n"
      "s_cselect_b32 s46, 0, s49\n"
      "buffer_load_dwordx4 v[52:55], %[vo], s[44:47], s53 offen nt\n"
      "s_bitcmp1_b32 s54, 0\n"
      "s_cselect_b32 s46, 0, s49\n"
      "buffer_load_dwordx4 v[56:59], %[vo], s[44:47], s54 offen nt\n"
      "s_bitcmp1_b32 s55, 0\n"
      "s_cselect_b32 s46, 0, s49\n"
      "buffer_load_dwordx4 v[60:63], %[vo], s[44:47], s55 offen nt\n"
      "s_cmp_lg_u32 s68, 0\n"
      "s_cbranch_scc1 1b\n"
      "3:\n"
      "v_mov_b32 v96, 0\n"
      "v_mov_b32 v97, 0\n"
      "v_mov_b32 v98, 0\n"
      "v_mov_b32 v99, 0\n"
      "v_mov_b32 v100, 0\n"
      "v_mov_b32 v101, 0\n"
      "v_mov_b32 v102, 0\n"
      "v_mov_b32 v103, 0\n"
      "v_mov_b32 v104, 0\n"
      "v_mov_b32 v105, 0\n"
      "v_mov_b32 v106, 0\n"
      "v_mov_b32 v107, 0\n"
      "v_mov_b32 v108, 0\n"
      "v_mov_b32 v109, 0\n"
      "v_mov_b32 v110, 0\n"
      "v_mov_b32 v111, 0\n"
      "v_mov_b32 v112, 0\n"
      "v_mov_b32 v113, 0\n"
      "v_mov_b32 v114, 0\n"
      "v_mov_b32 v115, 0\n"
      "v_mov_b32 v116, 0\n"
      "v_mov_b32 v117, 0\n"
      "v_mov_b32 v118, 0\n"
      "v_mov_b32 v119, 0\n"
      "v_mov_b32 v120, 0\n"
      "v_mov_b32 v121, 0\n"
      "v_mov_b32 v122, 0\n"
      "v_mov_b32 v123, 0\n"
      "v_mov_b32 v124, 0\n"
      "v_mov_b32 v125, 0\n"
      "v_mov_b32 v126, 0\n"
      "v_mov_b32 v127, 0\n"
      "v_mov_b32 v28, 0xff800000\n"
      "v_mov_b32 v29, 0x7f800000\n"
      "v_mov_b32 v30, 0\n"
      "s_add_u32 s68, s68, 1\n"
      "s_mov_b32 s48, 0\n"
      "s_cmp_eq_u32 s67, 0\n"
      "s_cbranch_scc1 8f\n"
      "s_waitcnt vmcnt(8)\n"
      "v_mov_b32 v20, 0\n"
      "v_mov_b32 v21, 0\n"
      "v_mov_b32 v22, 0\n"
      "v_mov_b32 v23, 0\n"
      "8:\n"
      "s_waitcnt vmcnt(4)\n"
      "s_memrealtime s[80:81]\n"
      "s_cmp_eq_u32 s60, 0\n"
      "s_cbranch_scc1 6f\n"
      "4:\n"
      "s_add_u32 s48, s48, 1\n"
      "s_cmp_lt_u32 s48, s60\n"
      "s_cselect_b32 s49, s50, 0\n"
      "s_lshl_b32 s51, s48, 2\n"
      "v_readlane_b32 s52, v27, s51\n"
      "s_add_u32 s51, s51, 1\n"
      "v_readlane_b32 s53, v27, s51\n"
      "s_add_u32 s51, s51, 1\n"
      "v_readlane_b32 s54, v27, s51\n"
      "s_add_u32 s51, s51, 1\n"
      "v_readlane_b32 s55, v27, s51\n"
      "s_waitcnt vmcnt(4)\n"
      "v_mfma_f32_16x16x32_bf16 v[64:67], v[32:35], v[20:23], 0\n"
      "v_mfma_f32_16x16x32_bf16 v[68:71], v[36:39], v[20:23], 0\n"
      "v_mfma_f32_16x16x32_bf16 v[72:75], v[40:43], v[20:23], 0\n"
      "v_mfma_f32_16x16x32_bf16 v[76:79], v[44:47], v[20:23], 0\n"
      "s_bitcmp1_b32 s52, 0\n"
      "s_cselect_b32 s42, 0, s49\n"
      "buffer_load_dwordx4 v[32:35], %[ko], s[40:43], s52 offen nt\n"
      "s_bitcmp1_b32 s53, 0\n"
      "s_cselect_b32 s42, 0, s49\n"
      "buffer_load_dwordx4 v[36:39], %[ko], s[40:43], s53 offen nt\n"
      "s_bitcmp1_b32 s54, 0\n"
      "s_cselect_b32 s42, 0, s49\n"
      "buffer_load_dwordx4 v[40:43], %[ko], s[40:43], s54 offen nt\n"
      "s_bitcmp1_b32 s55, 0\n"
      "s_cselect_b32 s42, 0, s49\n"
      "buffer_load_dwordx4 v[44:47], %[ko], s[40:43], s55 offen nt\n"
      "v_mov_b32_dpp v64, v64 quad_perm:[0,0,0,0] row_mask:0xf bank_mask:0xf\n"
      "v_mov_b32_dpp v68, v68 quad_perm:[0,0,0,0] row_mask:0xf bank_mask:0xf\n"
      "v_mov_b32_dpp v72, v72 quad_perm:[0,0,0,0] row_mask:0xf bank_mask:0xf\n"
      "v_mov_b32_dpp v76, v76 quad_perm:[0,0,0,0] row_mask:0xf bank_mask:0xf\n"
      "v_add_f32_dpp v64, v65, v64 quad_perm:[1,1,1,1] row_mask:0xf bank_mask:0xf\n"
      "v_add_f32_dpp v68, v69, v68 quad_perm:[1,1,1,1] row_mask:0xf bank_mask:0xf\n"
      "v_add_f32_dpp v72, v73, v72 quad_perm:[1,1,1,1] row_mask:0xf bank_mask:0xf\n"
      "v_add_f32_dpp v76, v77, v76 quad_perm:[1,1,1,1] row_mask:0xf bank_mask:0xf\n"
      "v_add_f32_dpp v64, v66, v64 quad_perm:[2,2,2,2] row_mask:0xf bank_mask:0xf\n"
      "v_add_f32_dpp v68, v70, v68 quad_perm:[2,2,2,2] row_mask:0xf bank_mask:0xf\n"
      "v_add_f32_dpp v72, v74, v72 quad_perm:[2,2,2,2] row_mask:0xf bank_mask:0xf\n"
      "v_add_f32_dpp v76, v78, v76 quad_perm:[2,2,2,2] row_mask:0xf bank_mask:0xf\n"
      "v_add_f32_dpp v64, v67, v64 quad_perm:[3,3,3,3] row_mask:0xf bank_mask:0xf\n"
      "v_add_f32_dpp v68, v71, v68 quad_perm:[3,3,3,3] row_mask:0xf bank_mask:0xf\n"
      "v_add_f32_dpp v72, v75, v72 quad_perm:[3,3,3,3] row_mask:0xf bank_mask:0xf\n"
      "v_add_f32_dpp v76, v79, v76 quad_perm:[3,3,3,3] row_mask:0xf bank_mask:0xf\n"
      "s_cmp_eq_u32 s48, s68\n"
      "s_cbranch_scc1 10f\n"
      "11:\n"
      "v_max3_f32 v65, v64, v68, v72\n"
      "v_max_f32 v65, v65, v76\n"
      "v_cmp_gt_f32 vcc, v65, v28\n"
      "s_cmp_lg_u64 vcc, 0\n"
      "s_cbranch_scc1 12f\n"
      "13:\n"
      "v_fma_f32 v65, v64, s63, v29\n"
      "v_fma_f32 v69, v68, s63, v29\n"
      "v_fma_f32 v73, v72, s63, v29\n"
      "v_fma_f32 v77, v76, s63, v29\n"
      "v_exp_f32 v65, v65\n"
      "v_exp_f32 v69, v69\n"
      "v_exp_f32 v73, v73\n"
      "v_exp_f32 v77, v77\n"
      "v_cvt_pk_bf16_f32 v66, v65, v69\n"
      "v_add_f32 v64, v65, v69\n"
      "v_cvt_pk_bf16_f32 v67, v73, v77\n"
      "v_add_f32 v68, v73, v77\n"
      "v_add_f32 v30, v30, v64\n"
      "v_add_f32 v30, v30, v68\n"
      "s_waitcnt vmcnt(4)\n"
      "v_perm_b32 v80, v52, v48, s61\n"
      "v_perm_b32 v81, v60, v56, s61\n"
      "v_perm_b32 v82, v52, v48, s62\n"
      "v_perm_b32 v83, v60, v56, s62\n"
      "v_perm_b32 v84, v53, v49, s61\n"
      "v_perm_b32 v85, v61, v57, s61\n"
      "v_perm_b32 v86, v53, v49, s62\n"
      "v_perm_b32 v87, v61, v57, s62\n"
      "v_perm_b32 v88, v54, v50, s61\n"
      "v_perm_b32 v89, v62, v58, s61\n"
      "v_perm_b32 v90, v54, v50, s62\n"
      "v_perm_b32 v91, v62, v58, s62\n"
      "v_perm_b32 v92, v55, v51, s61\n"
      "v_perm_b32 v93, v63, v59, s61\n"
      "v_perm_b32 v94, v55, v51, s62\n"
      "v_perm_b32 v95, v63, v59, s62\n"
      "s_bitcmp1_b32 s52, 0\n"
      "s_cselect_b32 s46, 0, s49\n"
      "buffer_load_dwordx4 v[48:51], %[vo], s[44:47], s52 offen nt\n"
      "s_bitcmp1_b32 s53, 0\n"
      "s_cselect_b32 s46, 0, s49\n"
      "buffer_load_dwordx4 v[52:55], %[vo], s[44:47], s53 offen nt\n"
      "s_bitcmp1_b32 s54, 0\n"
      "s_cselect_b32 s46, 0, s49\n"
      "buffer_load_dwordx4 v[56:59], %[vo], s[44:47], s54 offen nt\n"
      "s_bitcmp1_b32 s55, 0\n"
      "s_cselect_b32 s46, 0, s49\n"
      "buffer_load_dwordx4 v[60:63], %[vo], s[44:47], s55 offen nt\n"
      "v_mfma_f32_16x16x16_bf16 v[96:99], v[66:67], v[80:81], v[96:99]\n"
      "v_mfma_f32_16x16x16_bf16 v[100:103], v[66:67], v[82:83], v[100:103]\n"
      "v_mfma_f32_16x16x16_bf16 v[104:107], v[66:67], v[84:85], v[104:107]\n"
      "v_mfma_f32_16x16x16_bf16 v[108:111], v[66:67], v[86:87], v[108:111]\n"
      "v_mfma_f32_16x16x16_bf16 v[112:115], v[66:67], v[88:89], v[112:115]\n"
      "v_mfma_f32_16x16x16_bf16 v[116:119], v[66:67], v[90:91], v[116:119]\n"
      "v_mfma_f32_16x16x16_bf16 v[120:123], v[66:67], v[92:93], v[120:123]\n"
      "v_mfma_f32_16x16x16_bf16 v[124:127], v[66:67], v[94:95], v[124:127]\n"
      "s_cmp_lt_u32 s48, s60\n"
      "s_cbranch_scc1 4b\n"
      "s_branch 6f\n"
      "10:\n"
      "v_mov_b32 v69, 0xff800000\n"
      "v_cmp_gt_u32 vcc, s56, %[rr]\n"
      "s_nop 1\n"
      "v_cndmask_b32 v64, v69, v64, vcc\n"
      "v_cmp_gt_u32 vcc, s57, %[rr]\n"
      "s_nop 1\n"
      "v_cndmask_b32 v68, v69, v68, vcc\n"
      "v_cmp_gt_u32 vcc, s58, %[rr]\n"
      "s_nop 1\n"
      "v_cndmask_b32 v72, v69, v72, vcc\n"
      "v_cmp_gt_u32 vcc, s59, %[rr]\n"
      "s_nop 1\n"
      "v_cndmask_b32 v76, v69, v76, vcc\n"
      "s_branch 11b\n"
      "12:\n"
      "v_mov_b32 v69, v65\n"
      "s_nop 1\n"
      "v_permlane16_swap_b32 v65, v69\n"
      "s_nop 1\n"
      "v_max_f32 v65, v65, v69\n"
      "v_mov_b32 v69, v65\n"
      "s_nop 1\n"
      "v_permlane32_swap_b32 v65, v69\n"
      "s_nop 1\n"
      "v_max3_f32 v28, v65, v69, v28\n"
      "v_mul_f32 v65, s63, v28\n"
      "v_add_f32 v69, v65, v29\n"
      "v_exp_f32_e64 v69, -v69\n"
      "v_sub_f32 v29, 0, v65\n"
      "v_mul_f32 v30, v30, v69\n"
      "s_nop 1\n"
      "v_mov_b32_dpp v73, v69 row_newbcast:0 row_mask:0x1 bank_mask:0xf\n"
      "s_nop 1\n"
      "v_mov_b32_dpp v73, v69 row_newbcast:4 row_mask:0x2 bank_mask:0xf\n"
      "s_nop 1\n"
      "v_mov_b32_dpp v73, v69 row_newbcast:8 row_mask:0x4 bank_mask:0xf\n"
      "s_nop 1\n"
      "v_mov_b32_dpp v73, v69 row_newbcast:12 row_mask:0x8 bank_mask:0xf\n"
      "s_nop 1\n"
      "v_mul_f32 v96, v73, v96\n"
      "v_mul_f32 v100, v73, v100\n"
      "v_mul_f32 v104, v73, v104\n"
      "v_mul_f32 v108, v73, v108\n"
      "v_mul_f32 v112, v73, v112\n"
      "v_mul_f32 v116, v73, v116\n"
      "v_mul_f32 v120, v73, v120\n"
      "v_mul_f32 v124, v73, v124\n"
      "s_branch 13b\n"
      "6:\n"
      "s_waitcnt vmcnt(0) lgkmcnt(0)\n"
      "s_nop 7\n"
      : "={v96}"(o0), "={v100}"(o1), "={v104}"(o2), "={v108}"(o3), "={v112}"(o4), "={v116}"(o5), "={v120}"(o6), "={v124}"(o7),
        "={v29}"(nmc), "={v30}"(l), "={v31}"(epoch), "={s[78:79]}"(ts[7]), "={s[80:81]}"(tfirst)
      : [bto] "{v16}"(bto), [qo] "{v17}"(qo), [ko] "{v18}"(ko), [vo] "{v19}"(vo), [jl] "{v24}"(lane >> 2), [iu] "{v25}"((lane & 3) * 8192), [rr] "{v26}"(r),
        [bt] "s"(bt), [lens] "s"(lens), [qp] "s"(q), [ep] "s"(ep), [p0] "s"(p0), "s"(out),
        [klo] "s"((unsigned)ka), [khi] "s"((unsigned)(ka >> 32) & 0xffffu), [vlo] "s"((unsigned)va), [vhi] "s"((unsigned)(va >> 32) & 0xffffu)
      :
        "s40", "s41", "s42", "s43", "s44", "s45", "s46", "s47", "s48", "s49", "s50", "s51", "s52", "s53", "s54", "s55", "s56", "s57", "s58", "s59",
        "s60", "s61", "s62", "s63", "s64", "s65", "s66", "s67", "s68", "s69", "s70", "s71", "s72", "s73", "s74", "s75", "s76", "s77", "vcc", "scc",
        "m0", "memory", "v20", "v21", "v22", "v23", "v27", "v28", "v32", "v33", "v34", "v35", "v36", "v37", "v38", "v39", "v40", "v41", "v42", "v43",
        "v44", "v45", "v46", "v47", "v48", "v49", "v50", "v51", "v52", "v53", "v54", "v55", "v56", "v57", "v58", "v59", "v60", "v61", "v62", "v63",
        "v64", "v65", "v66", "v67", "v68", "v69", "v70", "v71", "v72", "v73", "v74", "v75", "v76", "v77", "v78", "v79", "v80", "v81", "v82", "v83",
        "v84", "v85", "v86", "v87", "v88", "v89", "v90", "v91", "v92", "v93", "v94", "v95", "v97", "v98", "v99", "v101", "v102", "v103", "v105",
        "v106", "v107", "v109", "v110", "v111", "v113", "v114", "v115", "v117", "v118", "v119", "v121", "v122", "v123", "v125", "v126", "v127");
  STAMP(1);
  l = xr_sum(l);
  {
    float* rec = lds + (wave * 4 + r) * REC + 4 + c * 8;
    *reinterpret_cast<f32x4*>(rec) = f32x4{o0, o1, o2, o3};
    *reinterpret_cast<f32x4*>(rec + 4) = f32x4{o4, o5, o6, o7};
    if ((lane & 0x33) == 0) *reinterpret_cast<f32x2*>(lds + (wave * 4 + (c >> 2)) * REC) = f32x2{fmaxf(-nmc, -3.0e38f), l};
  }
  const unsigned tag = epoch * 0x9E3779B1u + 0x7F4A7C15u;
  __syncthreads();
  STAMP(2);
  {
    const int hl = tid >> 5, dd = tid & 31;
    const float* ra = lds + hl * REC;
    const float* rb = ra + 16 * REC;
    const f32x2 a = *reinterpret_cast<const f32x2*>(ra), b = *reinterpret_cast<const f32x2*>(rb);
    const f32x4 oa = *reinterpret_cast<const f32x4*>(ra + 4 + dd * 4), ob = *reinterpret_cast<const f32x4*>(rb + 4 + dd * 4);
    const float M = fmaxf(a[0], b[0]);
    const float wa = __builtin_amdgcn_exp2f(a[0] - M), wb = __builtin_amdgcn_exp2f(b[0] - M);
    const f32x4 O = wa * oa + wb * ob;
    u32x4 pay;
#pragma unroll
    for (int i = 0; i < 4; ++i) pay[i] = (__float_as_uint(O[i]) & 0xffffff00u) | ((tag >> (8 * i)) & 0xffu);
    const int H = g * 16 + hl;
    const unsigned po = WS_PAY + ((H * 8 + (dd >> 2)) * NSPLIT + s) * 64 + (dd & 3) * 16;
    asm volatile("global_store_dwordx4 %0, %1, %2 sc1\n s_nop 1" ::"v"(po), "v"(pay), "s"(ws) : "memory");
    if (dd == 0) {
      const u32x4 ml = {__float_as_uint(M), tag, __float_as_uint(wa * a[1] + wb * b[1]), tag};
      const unsigned mo = WS_ML + (H * NSPLIT + s) * 16;
      asm volatile("global_store_dwordx4 %0, %1, %2 sc1\n s_nop 1" ::"v"(mo), "v"(ml), "s"(ws) : "memory");
    }
  }
  STAMP(3);
  const int H = g * 16 + (s >> 3);
  u32x4 pay = {0, 0, 0, 0}, ml = {0, 0, 0, 0};
  {
    const unsigned po = WS_PAY + (H * 8 + (s & 7)) * NSPLIT * 64 + tid * 16, mo = WS_ML + (H * NSPLIT + (tid >> 2)) * 16;
    bool need = true;
#pragma nounroll
    for (int spin = 0; spin < (1 << 17); ++spin) {
      if (need) asm volatile("global_load_dwordx4 %0, %2, %4 sc1\n global_load_dwordx4 %1, %3, %4 sc1\n s_waitcnt vmcnt(0)"
                             : "+v"(pay), "+v"(ml) : "v"(po), "v"(mo), "s"(ws) : "memory");
      const unsigned got = __builtin_amdgcn_perm(__builtin_amdgcn_perm(pay[3], pay[2], 0x0400), __builtin_amdgcn_perm(pay[1], pay[0], 0x0400), 0x05040100);
      unsigned long long b = __ballot(got == tag && ml[1] == tag && ml[3] == tag);
      b &= b >> 1; b &= b >> 2; b &= 0x1111111111111111ull;
      if (b == 0x1111111111111111ull) break;
      need = ((b >> (lane & 60)) & 1) == 0;
      __builtin_amdgcn_s_sleep(SLEEP);
    }
  }
  STAMP(4);
  float* fin = lds + WAVES * 4 * REC;
  {
    const float m = __uint_as_float(ml[0]);
    float M = fmaxf(m, dpp<ROR4>(m)); M = fmaxf(M, dpp<ROR8>(M)); M = xr_max(M);
    const float w = __builtin_amdgcn_exp2f(m - M);
    pay = (pay & 0xffffff00u) | 0x80u;
    float v[5] = {w * __uint_as_float(pay[0]), w * __uint_as_float(pay[1]), w * __uint_as_float(pay[2]), w * __uint_as_float(pay[3]), w * __uint_as_float(ml[2])};
#pragma unroll
    for (int i = 0; i < 5; ++i) { v[i] += dpp<ROR4>(v[i]); v[i] += dpp<ROR8>(v[i]); v[i] = xr_sum(v[i]); }
    if (lane < 4) {
      *reinterpret_cast<f32x4*>(fin + wave * 20 + 4 + lane * 4) = f32x4{v[0], v[1], v[2], v[3]};
      if (lane == 0) *reinterpret_cast<f32x2*>(fin + wave * 20) = f32x2{M, v[4]};
    }
  }
  __syncthreads();
  STAMP(5);
  if (wave == 0) {
    const int w8 = lane >> 3, dp = lane & 7;
    const f32x2 a = *reinterpret_cast<const f32x2*>(fin + w8 * 20), o = *reinterpret_cast<const f32x2*>(fin + w8 * 20 + 4 + dp * 2);
    float M = fmaxf(a[0], dpp<ROR8>(a[0])); M = xr_max(M);
    const float w = __builtin_amdgcn_exp2f(a[0] - M);
    float v[3] = {w * o[0], w * o[1], w * a[1]};
#pragma unroll
    for (int i = 0; i < 3; ++i) { v[i] += dpp<ROR8>(v[i]); v[i] = xr_sum(v[i]); }
    const float inv = 1.0f / v[2];
    const bf16x2 res = {(__bf16)(v[0] * inv), (__bf16)(v[1] * inv)};
    if (lane < 8) out[H * 64 + (s & 7) * 8 + lane] = __builtin_bit_cast(unsigned, res);
    if (s == 0 && lane == 0) __hip_atomic_store(reinterpret_cast<unsigned*>(ep), epoch + 1, __ATOMIC_RELAXED, __HIP_MEMORY_SCOPE_AGENT);
  }
  STAMP(6);
  if (PROBE > 1 && blockIdx.x == PWG && wave == 0) {
    const unsigned long long iv = PROBE == 2 ? ts[6] - ts[0] : PROBE == 3 ? ts[1] - ts[0] : PROBE == 4 ? tfirst - ts[0] : PROBE == 5 ? ts[7] - ts[0] : PROBE == 6 ? ts[4] - ts[3] : ts[3] - ts[1];
    const unsigned long long until = ts[6] + 10 * (iv < 20000 ? iv : 20000);
    while (__builtin_amdgcn_s_memrealtime() < until) __builtin_amdgcn_s_sleep(1);
  }
}
}

extern "C" void kernel_launch(void* const* d_in, const int* in_sizes, int n_in, void* d_out, int out_size, void* d_ws, size_t ws_size,
                              hipStream_t stream) {
  attn_fused<<<GRID, WAVES * 64, 0, stream>>>((const unsigned char*)d_in[0], (const unsigned char*)d_in[1], (const unsigned char*)d_in[2],
                                              (const int*)d_in[3], (const int*)d_in[4], (unsigned*)d_out, (unsigned char*)d_ws);
}
```

```cpp
#include <hip/hip_runtime.h>
#include <hip/hip_bf16.h>
#include <stdint.h>
#define SLEEP 2
#define OUTPOL 0
#define POL 2

namespace {
constexpr int D = 128, WAVES = 8, HPW = 4, NG = 2, NSPLIT = 128, GRID = 256;
constexpr int REC = D + 4;
constexpr unsigned WS_ML = 4096, WS_PAY = WS_ML + 32 * NSPLIT * 16;
#define STAMP(i)

using u32x4 = __attribute__((ext_vector_type(4))) unsigned;
using f32x4 = __attribute__((ext_vector_type(4))) float;
using f32x2 = __attribute__((ext_vector_type(2))) float;
using bf16x2 = __attribute__((ext_vector_type(2))) __bf16;

template <int CTRL> __device__ __forceinline__ float dpp(float x) {
  return __builtin_bit_cast(float, __builtin_amdgcn_mov_dpp(__builtin_bit_cast(int, x), CTRL, 0xf, 0xf, true));
}
__device__ __forceinline__ float xr_max(float x) {
  auto s = __builtin_amdgcn_permlane16_swap(__float_as_uint(x), __float_as_uint(x), false, false);
  x = fmaxf(__uint_as_float(s[0]), __uint_as_float(s[1]));
  auto t = __builtin_amdgcn_permlane32_swap(__float_as_uint(x), __float_as_uint(x), false, false);
  return fmaxf(__uint_as_float(t[0]), __uint_as_float(t[1]));
}
__device__ __forceinline__ float xr_sum(float x) {
  auto s = __builtin_amdgcn_permlane16_swap(__float_as_uint(x), __float_as_uint(x), false, false);
  x = __uint_as_float(s[0]) + __uint_as_float(s[1]);
  auto t = __builtin_amdgcn_permlane32_swap(__float_as_uint(x), __float_as_uint(x), false, false);
  return __uint_as_float(t[0]) + __uint_as_float(t[1]);
}
constexpr int ROR4 = 0x124, ROR8 = 0x128;

__global__ void __launch_bounds__(WAVES * 64, 2)
attn_fused(const unsigned char* __restrict__ q, const unsigned char* __restrict__ kc, const unsigned char* __restrict__ vc,
           const int* __restrict__ bt, const int* __restrict__ lens, unsigned* __restrict__ out, unsigned char* __restrict__ ws) {
  __shared__ __attribute__((aligned(16))) float lds[WAVES * 4 * REC + WAVES * 20];
  STAMP(0);
  const int tid = threadIdx.x, lane = tid & 63, wave = __builtin_amdgcn_readfirstlane(tid >> 6);
  const int r = lane >> 4, c = lane & 15;
  const int g = blockIdx.x % NG, s = blockIdx.x / NG;
  const int head = g * HPW + (wave & 3), p0 = s * 2 + (wave >> 2);
  const int bto = (p0 + 256 * (lane >> 2)) * 4;
  const int qo = ((head * 4 + (c >> 2)) * D + (c & 3) * 32 + r * 8) * 2;
  const int ko = (c >> 2) * 2048 + (c & 3) * 64 + r * 16 + head * 256;
  const int vo = r * 2048 + c * 16 + head * 256;
  const uint64_t ka = (uint64_t)kc, va = (uint64_t)vc;
  unsigned char* ep = ws + g * 256;
  float o0, o1, o2, o3, o4, o5, o6, o7, nmc, l;
  unsigned epoch;
  asm volatile(
      "v_mov_b32 v31, 0\n"
      "global_load_dword v27, %[bto], %[bt]\n"
      "s_load_dword s66, %[lens], 0x0\n"
      "global_load_dwordx4 v[20:23], %[qo], %[qp]\n"
      "global_load_dword v31, v31, %[ep] sc1\n"
      "s_mov_b32 s40, %[klo]\n"
      "s_mov_b32 s41, %[khi]\n"
      "s_mov_b32 s43, 0x20000\n"
      "s_mov_b32 s44, %[vlo]\n"
      "s_mov_b32 s45, %[vhi]\n"
      "s_mov_b32 s47, 0x20000\n"
      "s_mov_b32 s50, 0x8020000\n"
      "s_mov_b32 s61, 0x05040100\n"
      "s_mov_b32 s62, 0x07060302\n"
      "s_mov_b32 s63, 0x3e0293ee\n"
      "s_waitcnt lgkmcnt(0)\n"
      "s_cmp_lt_i32 s66, 1\n"
      "s_cselect_b32 s67, 1, 0\n"
      "s_cselect_b32 s66, 0x10000, s66\n"
      "s_min_i32 s66, s66, 0x10000\n"
      "s_lshr_b32 s68, s66, 12\n"
      "s_and_b32 s69, s66, 0xfff\n"
      "s_add_u32 s70, s69, 3\n"
      "s_lshr_b32 s70, s70, 2\n"
      "s_mul_i32 s71, %[p0], s70\n"
      "s_add_u32 s72, s71, s70\n"
      "s_lshr_b32 s71, s71, 8\n"
      "s_lshr_b32 s72, s72, 8\n"
      "s_lshr_b32 s73, s71, 2\n"
      "s_lshl_b32 s74, s68, 8\n"
      "s_add_u32 s74, s74, s73\n"
      "s_min_u32 s75, s74, 0xfff\n"
      "s_lshl_b32 s75, s75, 2\n"
      "s_load_dword s64, %[bt], s75\n"
      "s_add_u32 s74, s74, 1\n"
      "s_min_u32 s74, s74, 0xfff\n"
      "s_lshl_b32 s74, s74, 2\n"
      "s_load_dword s65, %[bt], s74\n"
      "s_cmp_lt_u32 s71, s72\n"
      "s_addc_u32 s60, s68, 0\n"
      "v_cmp_gt_u32 vcc, s68, %[jl]\n"
      "s_cmp_lg_u32 s60, 0\n"
      "s_cselect_b32 s49, s50, 0\n"
      "s_waitcnt vmcnt(2)\n"
      "v_lshl_add_u32 v27, v27, 15, %[iu]\n"
      "v_cndmask_b32 v27, 1, v27, vcc\n"
      "s_cmp_lg_u32 s68, 0\n"
      "s_cbranch_scc1 2f\n"
      "1:\n"
      "s_waitcnt lgkmcnt(0)\n"
      "s_cmp_lt_u32 s71, s72\n"
      "s_cbranch_scc0 7f\n"
      "s_lshl_b32 s74, s68, 2\n"
      "s_add_u32 s75, s71, 0\n"
      "s_lshr_b32 s76, s75, 2\n"
      "s_cmp_eq_u32 s76, s73\n"
      "s_cselect_b32 s76, s64, s65\n"
      "s_lshl_b32 s76, s76, 15\n"
      "s_and_b32 s77, s75, 3\n"
      "s_lshl_b32 s77, s77, 13\n"
      "s_or_b32 s76, s76, s77\n"
      "s_lshl_b32 s77, s75, 2\n"
      "s_sub_u32 s77, s69, s77\n"
      "s_cmp_lt_u32 s75, s72\n"
      "s_cselect_b32 s76, s76, 1\n"
      "s_cselect_b32 s56, s77, 0\n"
      "s_add_u32 m0, s74, 0\n"
      "s_nop 0\n"
      "v_writelane_b32 v27, s76, m0\n"
      "s_add_u32 s75, s71, 1\n"
      "s_lshr_b32 s76, s75, 2\n"
      "s_cmp_eq_u32 s76, s73\n"
      "s_cselect_b32 s76, s64, s65\n"
      "s_lshl_b32 s76, s76, 15\n"
      "s_and_b32 s77, s75, 3\n"
      "s_lshl_b32 s77, s77, 13\n"
      "s_or_b32 s76, s76, s77\n"
      "s_lshl_b32 s77, s75, 2\n"
      "s_sub_u32 s77, s69, s77\n"
      "s_cmp_lt_u32 s75, s72\n"
      "s_cselect_b32 s76, s76, 1\n"
      "s_cselect_b32 s57, s77, 0\n"
      "s_add_u32 m0, s74, 1\n"
      "s_nop 0\n"
      "v_writelane_b32 v27, s76, m0\n"
      "s_add_u32 s75, s71, 2\n"
      "s_lshr_b32 s76, s75, 2\n"
      "s_cmp_eq_u32 s76, s73\n"
      "s_cselect_b32 s76, s64, s65\n"
      "s_lshl_b32 s76, s76, 15\n"
      "s_and_b32 s77, s75, 3\n"
      "s_lshl_b32 s77, s77, 13\n"
      "s_or_b32 s76, s76, s77\n"
      "s_lshl_b32 s77, s75, 2\n"
      "s_sub_u32 s77, s69, s77\n"
      "s_cmp_lt_u32 s75, s72\n"
      "s_cselect_b32 s76, s76, 1\n"
      "s_cselect_b32 s58, s77, 0\n"
      "s_add_u32 m0, s74, 2\n"
      "s_nop 0\n"
      "v_writelane_b32 v27, s76, m0\n"
      "s_add_u32 s75, s71, 3\n"
      "s_lshr_b32 s76, s75, 2\n"
      "s_cmp_eq_u32 s76, s73\n"
      "s_cselect_b32 s76, s64, s65\n"
      "s_lshl_b32 s76, s76, 15\n"
      "s_and_b32 s77, s75, 3\n"
      "s_lshl_b32 s77, s77, 13\n"
      "s_or_b32 s76, s76, s77\n"
      "s_lshl_b32 s77, s75, 2\n"
      "s_sub_u32 s77, s69, s77\n"
      "s_cmp_lt_u32 s75, s72\n"
      "s_cselect_b32 s76, s76, 1\n"
      "s_cselect_b32 s59, s77, 0\n"
      "s_add_u32 m0, s74, 3\n"
      "s_nop 0\n"
      "v_writelane_b32 v27, s76, m0\n"
      "7:\n"
      "s_cmp_lg_u32 s68, 0\n"
      "s_cbranch_scc1 3f\n"
      "2:\n"
      "s_nop 0\n"
      "v_readlane_b32 s52, v27, 0\n"
      "v_readlane_b32 s53, v27, 1\n"
      "v_readlane_b32 s54, v27, 2\n"
      "v_readlane_b32 s55, v27, 3\n"
      "s_nop 2\n"
      "s_bitcmp1_b32 s52, 0\n"
      "s_cselect_b32 s42, 0, s49\n"
      "buffer_load_dwordx4 v[32:35], %[ko], s[40:43], s52 offen sc0 sc1 nt\n"
      "s_bitcmp1_b32 s53, 0\n"
      "s_cselect_b32 s42, 0, s49\n"
      "buffer_load_dwordx4 v[36:39], %[ko], s[40:43], s53 offen sc0 sc1 nt\n"
      "s_bitcmp1_b32 s54, 0\n"
      "s_cselect_b32 s42, 0, s49\n"
      "buffer_load_dwordx4 v[40:43], %[ko], s[40:43], s54 offen sc0 sc1 nt\n"
      "s_bitcmp1_b32 s55, 0\n"
      "s_cselect_b32 s42, 0, s49\n"
      "buffer_load_dwordx4 v[44:47], %[ko], s[40:43], s55 offen sc0 sc1 nt\n"
      "s_bitcmp1_b32 s52, 0\n"
      "s_cselect_b32 s46, 0, s49\n"
      "buffer_load_dwordx4 v[48:51], %[vo], s[44:47], s52 offen sc0 sc1 nt\n"
      "s_bitcmp1_b32 s53, 0\n"
      "s_cselect_b32 s46, 0, s49\n"
      "buffer_load_dwordx4 v[52:55], %[vo], s[44:47], s53 offen sc0 sc1 nt\n"
      "s_bitcmp1_b32 s54, 0\n"
      "s_cselect_b32 s46, 0, s49\n"
      "buffer_load_dwordx4 v[56:59], %[vo], s[44:47], s54 offen sc0 sc1 nt\n"
      "s_bitcmp1_b32 s55, 0\n"
      "s_cselect_b32 s46, 0, s49\n"
      "buffer_load_dwordx4 v[60:63], %[vo], s[44:47], s55 offen sc0 sc1 nt\n"
      "s_cmp_lg_u32 s68, 0\n"
      "s_cbranch_scc1 1b\n"
      "3:\n"
      "v_mov_b32 v96, 0\n"
      "v_mov_b32 v97, 0\n"
      "v_mov_b32 v98, 0\n"
      "v_mov_b32 v99, 0\n"
      "v_mov_b32 v100, 0\n"
      "v_mov_b32 v101, 0\n"
      "v_mov_b32 v102, 0\n"
      "v_mov_b32 v103, 0\n"
      "v_mov_b32 v104, 0\n"
      "v_mov_b32 v105, 0\n"
      "v_mov_b32 v106, 0\n"
      "v_mov_b32 v107, 0\n"
      "v_mov_b32 v108, 0\n"
      "v_mov_b32 v109, 0\n"
      "v_mov_b32 v110, 0\n"
      "v_mov_b32 v111, 0\n"
      "v_mov_b32 v112, 0\n"
      "v_mov_b32 v113, 0\n"
      "v_mov_b32 v114, 0\n"
      "v_mov_b32 v115, 0\n"
      "v_mov_b32 v116, 0\n"
      "v_mov_b32 v117, 0\n"
      "v_mov_b32 v118, 0\n"
      "v_mov_b32 v119, 0\n"
      "v_mov_b32 v120, 0\n"
      "v_mov_b32 v121, 0\n"
      "v_mov_b32 v122, 0\n"
      "v_mov_b32 v123, 0\n"
      "v_mov_b32 v124, 0\n"
      "v_mov_b32 v125, 0\n"
      "v_mov_b32 v126, 0\n"
      "v_mov_b32 v127, 0\n"
      "v_mov_b32 v28, 0xff800000\n"
      "v_mov_b32 v29, 0x7f800000\n"
      "v_mov_b32 v30, 0\n"
      "s_add_u32 s68, s68, 1\n"
      "s_mov_b32 s48, 0\n"
      "s_cmp_eq_u32 s67, 0\n"
      "s_cbranch_scc1 8f\n"
      "s_waitcnt vmcnt(8)\n"
      "v_mov_b32 v20, 0\n"
      "v_mov_b32 v21, 0\n"
      "v_mov_b32 v22, 0\n"
      "v_mov_b32 v23, 0\n"
      "8:\n"
      "s_cmp_eq_u32 s60, 0\n"
      "s_cbranch_scc1 6f\n"
      "4:\n"
      "s_add_u32 s48, s48, 1\n"
      "s_cmp_lt_u32 s48, s60\n"
      "s_cselect_b32 s49, s50, 0\n"
      "s_lshl_b32 s51, s48, 2\n"
      "v_readlane_b32 s52, v27, s51\n"
      "s_add_u32 s51, s51, 1\n"
      "v_readlane_b32 s53, v27, s51\n"
      "s_add_u32 s51, s51, 1\n"
      "v_readlane_b32 s54, v27, s51\n"
      "s_add_u32 s51, s51, 1\n"
      "v_readlane_b32 s55, v27, s51\n"
      "s_waitcnt vmcnt(4)\n"
      "v_mfma_f32_16x16x32_bf16 v[64:67], v[32:35], v[20:23], 0\n"
      "v_mfma_f32_16x16x32_bf16 v[68:71], v[36:39], v[20:23], 0\n"
      "v_mfma_f32_16x16x32_bf16 v[72:75], v[40:43], v[20:23], 0\n"
      "v_mfma_f32_16x16x32_bf16 v[76:79], v[44:47], v[20:23], 0\n"
      "s_bitcmp1_b32 s52, 0\n"
      "s_cselect_b32 s42, 0, s49\n"
      "buffer_load_dwordx4 v[32:35], %[ko], s[40:43], s52 offen sc0 sc1 nt\n"
      "s_bitcmp1_b32 s53, 0\n"
      "s_cselect_b32 s42, 0, s49\n"
      "buffer_load_dwordx4 v[36:39], %[ko], s[40:43], s53 offen sc0 sc1 nt\n"
      "s_bitcmp1_b32 s54, 0\n"
      "s_cselect_b32 s42, 0, s49\n"
      "buffer_load_dwordx4 v[40:43], %[ko], s[40:43], s54 offen sc0 sc1 nt\n"
      "s_bitcmp1_b32 s55, 0\n"
      "s_cselect_b32 s42, 0, s49\n"
      "buffer_load_dwordx4 v[44:47], %[ko], s[40:43], s55 offen sc0 sc1 nt\n"
      "v_mov_b32_dpp v64, v64 quad_perm:[0,0,0,0] row_mask:0xf bank_mask:0xf\n"
      "v_mov_b32_dpp v68, v68 quad_perm:[0,0,0,0] row_mask:0xf bank_mask:0xf\n"
      "v_mov_b32_dpp v72, v72 quad_perm:[0,0,0,0] row_mask:0xf bank_mask:0xf\n"
      "v_mov_b32_dpp v76, v76 quad_perm:[0,0,0,0] row_mask:0xf bank_mask:0xf\n"
      "v_add_f32_dpp v64, v65, v64 quad_perm:[1,1,1,1] row_mask:0xf bank_mask:0xf\n"
      "v_add_f32_dpp v68, v69, v68 quad_perm:[1,1,1,1] row_mask:0xf bank_mask:0xf\n"
      "v_add_f32_dpp v72, v73, v72 quad_perm:[1,1,1,1] row_mask:0xf bank_mask:0xf\n"
      "v_add_f32_dpp v76, v77, v76 quad_perm:[1,1,1,1] row_mask:0xf bank_mask:0xf\n"
      "v_add_f32_dpp v64, v66, v64 quad_perm:[2,2,2,2] row_mask:0xf bank_mask:0xf\n"
      "v_add_f32_dpp v68, v70, v68 quad_perm:[2,2,2,2] row_mask:0xf bank_mask:0xf\n"
      "v_add_f32_dpp v72, v74, v72 quad_perm:[2,2,2,2] row_mask:0xf bank_mask:0xf\n"
      "v_add_f32_dpp v76, v78, v76 quad_perm:[2,2,2,2] row_mask:0xf bank_mask:0xf\n"
      "v_add_f32_dpp v64, v67, v64 quad_perm:[3,3,3,3] row_mask:0xf bank_mask:0xf\n"
      "v_add_f32_dpp v68, v71, v68 quad_perm:[3,3,3,3] row_mask:0xf bank_mask:0xf\n"
      "v_add_f32_dpp v72, v75, v72 quad_perm:[3,3,3,3] row_mask:0xf bank_mask:0xf\n"
      "v_add_f32_dpp v76, v79, v76 quad_perm:[3,3,3,3] row_mask:0xf bank_mask:0xf\n"
      "s_cmp_eq_u32 s48, s68\n"
      "s_cbranch_scc1 10f\n"
      "11:\n"
      "v_max3_f32 v65, v64, v68, v72\n"
      "v_max_f32 v65, v65, v76\n"
      "v_cmp_gt_f32 vcc, v65, v28\n"
      "s_cmp_lg_u64 vcc, 0\n"
      "s_cbranch_scc1 12f\n"
      "13:\n"
      "v_fma_f32 v65, v64, s63, v29\n"
      "v_fma_f32 v69, v68, s63, v29\n"
      "v_fma_f32 v73, v72, s63, v29\n"
      "v_fma_f32 v77, v76, s63, v29\n"
      "v_exp_f32 v65, v65\n"
      "v_exp_f32 v69, v69\n"
      "v_exp_f32 v73, v73\n"
      "v_exp_f32 v77, v77\n"
      "v_cvt_pk_bf16_f32 v66, v65, v69\n"
      "v_add_f32 v64, v65, v69\n"
      "v_cvt_pk_bf16_f32 v67, v73, v77\n"
      "v_add_f32 v68, v73, v77\n"
      "v_add_f32 v30, v30, v64\n"
      "v_add_f32 v30, v30, v68\n"
      "s_waitcnt vmcnt(4)\n"
      "v_perm_b32 v80, v52, v48, s61\n"
      "v_perm_b32 v81, v60, v56, s61\n"
      "v_perm_b32 v82, v52, v48, s62\n"
      "v_perm_b32 v83, v60, v56, s62\n"
      "v_perm_b32 v84, v53, v49, s61\n"
      "v_perm_b32 v85, v61, v57, s61\n"
      "v_perm_b32 v86, v53, v49, s62\n"
      "v_perm_b32 v87, v61, v57, s62\n"
      "v_perm_b32 v88, v54, v50, s61\n"
      "v_perm_b32 v89, v62, v58, s61\n"
      "v_perm_b32 v90, v54, v50, s62\n"
      "v_perm_b32 v91, v62, v58, s62\n"
      "v_perm_b32 v92, v55, v51, s61\n"
      "v_perm_b32 v93, v63, v59, s61\n"
      "v_perm_b32 v94, v55, v51, s62\n"
      "v_perm_b32 v95, v63, v59, s62\n"
      "s_bitcmp1_b32 s52, 0\n"
      "s_cselect_b32 s46, 0, s49\n"
      "buffer_load_dwordx4 v[48:51], %[vo], s[44:47], s52 offen sc0 sc1 nt\n"
      "s_bitcmp1_b32 s53, 0\n"
      "s_cselect_b32 s46, 0, s49\n"
      "buffer_load_dwordx4 v[52:55], %[vo], s[44:47], s53 offen sc0 sc1 nt\n"
      "s_bitcmp1_b32 s54, 0\n"
      "s_cselect_b32 s46, 0, s49\n"
      "buffer_load_dwordx4 v[56:59], %[vo], s[44:47], s54 offen sc0 sc1 nt\n"
      "s_bitcmp1_b32 s55, 0\n"
      "s_cselect_b32 s46, 0, s49\n"
      "buffer_load_dwordx4 v[60:63], %[vo], s[44:47], s55 offen sc0 sc1 nt\n"
      "v_mfma_f32_16x16x16_bf16 v[96:99], v[66:67], v[80:81], v[96:99]\n"
      "v_mfma_f32_16x16x16_bf16 v[100:103], v[66:67], v[82:83], v[100:103]\n"
      "v_mfma_f32_16x16x16_bf16 v[104:107], v[66:67], v[84:85], v[104:107]\n"
      "v_mfma_f32_16x16x16_bf16 v[108:111], v[66:67], v[86:87], v[108:111]\n"
      "v_mfma_f32_16x16x16_bf16 v[112:115], v[66:67], v[88:89], v[112:115]\n"
      "v_mfma_f32_16x16x16_bf16 v[116:119], v[66:67], v[90:91], v[116:119]\n"
      "v_mfma_f32_16x16x16_bf16 v[120:123], v[66:67], v[92:93], v[120:123]\n"
      "v_mfma_f32_16x16x16_bf16 v[124:127], v[66:67], v[94:95], v[124:127]\n"
      "s_cmp_lt_u32 s48, s60\n"
      "s_cbranch_scc1 4b\n"
      "s_branch 6f\n"
      "10:\n"
      "v_mov_b32 v69, 0xff800000\n"
      "v_cmp_gt_u32 vcc, s56, %[rr]\n"
      "s_nop 1\n"
      "v_cndmask_b32 v64, v69, v64, vcc\n"
      "v_cmp_gt_u32 vcc, s57, %[rr]\n"
      "s_nop 1\n"
      "v_cndmask_b32 v68, v69, v68, vcc\n"
      "v_cmp_gt_u32 vcc, s58, %[rr]\n"
      "s_nop 1\n"
      "v_cndmask_b32 v72, v69, v72, vcc\n"
      "v_cmp_gt_u32 vcc, s59, %[rr]\n"
      "s_nop 1\n"
      "v_cndmask_b32 v76, v69, v76, vcc\n"
      "s_branch 11b\n"
      "12:\n"
      "v_mov_b32 v69, v65\n"
      "s_nop 1\n"
      "v_permlane16_swap_b32 v65, v69\n"
      "s_nop 1\n"
      "v_max_f32 v65, v65, v69\n"
      "v_mov_b32 v69, v65\n"
      "s_nop 1\n"
      "v_permlane32_swap_b32 v65, v69\n"
      "s_nop 1\n"
      "v_max3_f32 v28, v65, v69, v28\n"
      "v_mul_f32 v65, s63, v28\n"
      "v_add_f32 v69, v65, v29\n"
      "v_exp_f32_e64 v69, -v69\n"
      "v_sub_f32 v29, 0, v65\n"
      "v_mul_f32 v30, v30, v69\n"
      "s_nop 1\n"
      "v_mov_b32_dpp v73, v69 row_newbcast:0 row_mask:0x1 bank_mask:0xf\n"
      "s_nop 1\n"
      "v_mov_b32_dpp v73, v69 row_newbcast:4 row_mask:0x2 bank_mask:0xf\n"
      "s_nop 1\n"
      "v_mov_b32_dpp v73, v69 row_newbcast:8 row_mask:0x4 bank_mask:0xf\n"
      "s_nop 1\n"
      "v_mov_b32_dpp v73, v69 row_newbcast:12 row_mask:0x8 bank_mask:0xf\n"
      "s_nop 1\n"
      "v_mul_f32 v96, v73, v96\n"
      "v_mul_f32 v100, v73, v100\n"
      "v_mul_f32 v104, v73, v104\n"
      "v_mul_f32 v108, v73, v108\n"
      "v_mul_f32 v112, v73, v112\n"
      "v_mul_f32 v116, v73, v116\n"
      "v_mul_f32 v120, v73, v120\n"
      "v_mul_f32 v124, v73, v124\n"
      "s_branch 13b\n"
      "6:\n"
      "s_waitcnt vmcnt(0) lgkmcnt(0)\n"
      "s_nop 7\n"
      : "={v96}"(o0), "={v100}"(o1), "={v104}"(o2), "={v108}"(o3), "={v112}"(o4), "={v116}"(o5), "={v120}"(o6), "={v124}"(o7),
        "={v29}"(nmc), "={v30}"(l), "={v31}"(epoch)
      : [bto] "{v16}"(bto), [qo] "{v17}"(qo), [ko] "{v18}"(ko), [vo] "{v19}"(vo), [jl] "{v24}"(lane >> 2), [iu] "{v25}"((lane & 3) * 8192), [rr] "{v26}"(r),
        [bt] "s"(bt), [lens] "s"(lens), [qp] "s"(q), [ep] "s"(ep), [p0] "s"(p0), "s"(out),
        [klo] "s"((unsigned)ka), [khi] "s"((unsigned)(ka >> 32) & 0xffffu), [vlo] "s"((unsigned)va), [vhi] "s"((unsigned)(va >> 32) & 0xffffu)
      :
        "s40", "s41", "s42", "s43", "s44", "s45", "s46", "s47", "s48", "s49", "s50", "s51", "s52", "s53", "s54", "s55", "s56", "s57", "s58", "s59",
        "s60", "s61", "s62", "s63", "s64", "s65", "s66", "s67", "s68", "s69", "s70", "s71", "s72", "s73", "s74", "s75", "s76", "s77", "vcc", "scc",
        "m0", "memory", "v20", "v21", "v22", "v23", "v27", "v28", "v32", "v33", "v34", "v35", "v36", "v37", "v38", "v39", "v40", "v41", "v42", "v43",
        "v44", "v45", "v46", "v47", "v48", "v49", "v50", "v51", "v52", "v53", "v54", "v55", "v56", "v57", "v58", "v59", "v60", "v61", "v62", "v63",
        "v64", "v65", "v66", "v67", "v68", "v69", "v70", "v71", "v72", "v73", "v74", "v75", "v76", "v77", "v78", "v79", "v80", "v81", "v82", "v83",
        "v84", "v85", "v86", "v87", "v88", "v89", "v90", "v91", "v92", "v93", "v94", "v95", "v97", "v98", "v99", "v101", "v102", "v103", "v105",
        "v106", "v107", "v109", "v110", "v111", "v113", "v114", "v115", "v117", "v118", "v119", "v121", "v122", "v123", "v125", "v126", "v127");
  STAMP(1);
  l = xr_sum(l);
  {
    float* rec = lds + (wave * 4 + r) * REC + 4 + c * 8;
    *reinterpret_cast<f32x4*>(rec) = f32x4{o0, o1, o2, o3};
    *reinterpret_cast<f32x4*>(rec + 4) = f32x4{o4, o5, o6, o7};
    if ((lane & 0x33) == 0) *reinterpret_cast<f32x2*>(lds + (wave * 4 + (c >> 2)) * REC) = f32x2{fmaxf(-nmc, -3.0e38f), l};
  }
  const unsigned tag = epoch * 0x9E3779B1u + 0x7F4A7C15u;
  __syncthreads();
  STAMP(2);
  {
    const int hl = tid >> 5, dd = tid & 31;
    const float* ra = lds + hl * REC;
    const float* rb = ra + 16 * REC;
    const f32x2 a = *reinterpret_cast<const f32x2*>(ra), b = *reinterpret_cast<const f32x2*>(rb);
    const f32x4 oa = *reinterpret_cast<const f32x4*>(ra + 4 + dd * 4), ob = *reinterpret_cast<const f32x4*>(rb + 4 + dd * 4);
    const float M = fmaxf(a[0], b[0]);
    const float wa = __builtin_amdgcn_exp2f(a[0] - M), wb = __builtin_amdgcn_exp2f(b[0] - M);
    const f32x4 O = wa * oa + wb * ob;
    u32x4 pay;
#pragma unroll
    for (int i = 0; i < 4; ++i) pay[i] = (__float_as_uint(O[i]) & 0xffffff00u) | ((tag >> (8 * i)) & 0xffu);
    const int H = g * 16 + hl;
    const unsigned po = WS_PAY + ((H * 8 + (dd >> 2)) * NSPLIT + s) * 64 + (dd & 3) * 16;
    asm volatile("global_store_dwordx4 %0, %1, %2 sc1\n s_nop 1" ::"v"(po), "v"(pay), "s"(ws) : "memory");
    if (dd == 0) {
      const u32x4 ml = {__float_as_uint(M), tag, __float_as_uint(wa * a[1] + wb * b[1]), tag};
      const unsigned mo = WS_ML + (H * NSPLIT + s) * 16;
      asm volatile("global_store_dwordx4 %0, %1, %2 sc1\n s_nop 1" ::"v"(mo), "v"(ml), "s"(ws) : "memory");
    }
  }
  STAMP(3);
  const int H = g * 16 + (s >> 3);
  u32x4 pay = {0, 0, 0, 0}, ml = {0, 0, 0, 0};
  {
    const unsigned po = WS_PAY + (H * 8 + (s & 7)) * NSPLIT * 64 + tid * 16, mo = WS_ML + (H * NSPLIT + (tid >> 2)) * 16;
    bool need = true;
#pragma nounroll
    for (int spin = 0; spin < (1 << 17); ++spin) {
      if (need) asm volatile("global_load_dwordx4 %0, %2, %4 sc1\n global_load_dwordx4 %1, %3, %4 sc1\n s_waitcnt vmcnt(0)"
                             : "+v"(pay), "+v"(ml) : "v"(po), "v"(mo), "s"(ws) : "memory");
      const unsigned got = __builtin_amdgcn_perm(__builtin_amdgcn_perm(pay[3], pay[2], 0x0400), __builtin_amdgcn_perm(pay[1], pay[0], 0x0400), 0x05040100);
      unsigned long long b = __ballot(got == tag && ml[1] == tag && ml[3] == tag);
      b &= b >> 1; b &= b >> 2; b &= 0x1111111111111111ull;
      if (b == 0x1111111111111111ull) break;
      need = ((b >> (lane & 60)) & 1) == 0;
      __builtin_amdgcn_s_sleep(SLEEP);
    }
  }
  STAMP(4);
  float* fin = lds + WAVES * 4 * REC;
  {
    const float m = __uint_as_float(ml[0]);
    float M = fmaxf(m, dpp<ROR4>(m)); M = fmaxf(M, dpp<ROR8>(M)); M = xr_max(M);
    const float w = __builtin_amdgcn_exp2f(m - M);
    pay = (pay & 0xffffff00u) | 0x80u;
    float v[5] = {w * __uint_as_float(pay[0]), w * __uint_as_float(pay[1]), w * __uint_as_float(pay[2]), w * __uint_as_float(pay[3]), w * __uint_as_float(ml[2])};
#pragma unroll
    for (int i = 0; i < 5; ++i) { v[i] += dpp<ROR4>(v[i]); v[i] += dpp<ROR8>(v[i]); v[i] = xr_sum(v[i]); }
    if (lane < 4) {
      *reinterpret_cast<f32x4*>(fin + wave * 20 + 4 + lane * 4) = f32x4{v[0], v[1], v[2], v[3]};
      if (lane == 0) *reinterpret_cast<f32x2*>(fin + wave * 20) = f32x2{M, v[4]};
    }
  }
  __syncthreads();
  STAMP(5);
  if (wave == 0) {
    const int w8 = lane >> 3, dp = lane & 7;
    const f32x2 a = *reinterpret_cast<const f32x2*>(fin + w8 * 20), o = *reinterpret_cast<const f32x2*>(fin + w8 * 20 + 4 + dp * 2);
    float M = fmaxf(a[0], dpp<ROR8>(a[0])); M = xr_max(M);
    const float w = __builtin_amdgcn_exp2f(a[0] - M);
    float v[3] = {w * o[0], w * o[1], w * a[1]};
#pragma unroll
    for (int i = 0; i < 3; ++i) { v[i] += dpp<ROR8>(v[i]); v[i] = xr_sum(v[i]); }
    const float inv = 1.0f / v[2];
    const bf16x2 res = {(__bf16)(v[0] * inv), (__bf16)(v[1] * inv)};
    if (lane < 8) out[H * 64 + (s & 7) * 8 + lane] = __builtin_bit_cast(unsigned, res);
    if (s == 0 && lane == 0) __hip_atomic_store(reinterpret_cast<unsigned*>(ep), epoch + 1, __ATOMIC_RELAXED, __HIP_MEMORY_SCOPE_AGENT);
  }
}
}

extern "C" void kernel_launch(void* const* d_in, const int* in_sizes, int n_in, void* d_out, int out_size, void* d_ws, size_t ws_size,
                              hipStream_t stream) {
  attn_fused<<<GRID, WAVES * 64, 0, stream>>>((const unsigned char*)d_in[0], (const unsigned char*)d_in[1], (const unsigned char*)d_in[2],
                                              (const int*)d_in[3], (const int*)d_in[4], (unsigned*)d_out, (unsigned char*)d_ws);
}
```

```cpp
#include <hip/hip_runtime.h>
#include <hip/hip_bf16.h>
#include <stdint.h>
#define SLEEP 2
#define WARM 0
#define LAY 2
#define TPOS 4
#define B 1
#define MAP 0
#define KPAT 1
#define POL 2

namespace {
constexpr int D = 128, WAVES = 8, HPW = 4, NG = 2, NSPLIT = 128, GRID = 256;
constexpr unsigned WS_PAY = 1u << 20;
__device__ __forceinline__ unsigned pay_at(int H, int k, int s) {
  return WS_PAY + ((H * 4 + (k >> 1)) * NSPLIT + s) * 128 + (k & 1) * 64;
}
constexpr int L_M = WAVES * 2048, L_L = L_M + 128, L_C = L_L + 512, L_F = L_C + 64, L_END = L_F + WAVES * 80;
#define STAMP(i)

using u32x4 = __attribute__((ext_vector_type(4))) unsigned;
using f32x4 = __attribute__((ext_vector_type(4))) float;
using f32x2 = __attribute__((ext_vector_type(2))) float;
using bf16x2 = __attribute__((ext_vector_type(2))) __bf16;

template <int CTRL> __device__ __forceinline__ float dpp(float x) {
  return __builtin_bit_cast(float, __builtin_amdgcn_mov_dpp(__builtin_bit_cast(int, x), CTRL, 0xf, 0xf, true));
}
__device__ __forceinline__ float xr_max(float x) {
  auto s = __builtin_amdgcn_permlane16_swap(__float_as_uint(x), __float_as_uint(x), false, false);
  x = fmaxf(__uint_as_float(s[0]), __uint_as_float(s[1]));
  auto t = __builtin_amdgcn_permlane32_swap(__float_as_uint(x), __float_as_uint(x), false, false);
  return fmaxf(__uint_as_float(t[0]), __uint_as_float(t[1]));
}
__device__ __forceinline__ float xr_sum(float x) {
  auto s = __builtin_amdgcn_permlane16_swap(__float_as_uint(x), __float_as_uint(x), false, false);
  x = __uint_as_float(s[0]) + __uint_as_float(s[1]);
  auto t = __builtin_amdgcn_permlane32_swap(__float_as_uint(x), __float_as_uint(x), false, false);
  return __uint_as_float(t[0]) + __uint_as_float(t[1]);
}
constexpr int ROR4 = 0x124, ROR8 = 0x128;
__device__ __forceinline__ unsigned tagged(float x, unsigned b) { return __builtin_amdgcn_perm(__float_as_uint(x), b, 0x07060500); }

__global__ void __launch_bounds__(WAVES * 64, 2)
attn_fused(const unsigned char* __restrict__ q, const unsigned char* __restrict__ kc, const unsigned char* __restrict__ vc,
           const int* __restrict__ bt, const int* __restrict__ lens, unsigned* __restrict__ out, unsigned char* __restrict__ ws) {
  __shared__ __attribute__((aligned(16))) float lds[L_END / 4];
  STAMP(0);
  const int tid = threadIdx.x, lane = tid & 63, wave = __builtin_amdgcn_readfirstlane(tid >> 6);
  const int r = lane >> 4, c = lane & 15;
  const int g = blockIdx.x % NG, s = blockIdx.x / NG;
  const int head = g * HPW + (wave & 3), p0 = s * 2 + (wave >> 2);
  const int bto = (p0 + 256 * (lane >> 2)) * 4;
  const int qo = ((head * 4 + (c >> 2)) * D + (c & 3) * 8 + r * 32) * 2;
  const int ko = (c >> 2) * 2048 + (c & 3) * 16 + r * 64 + head * 256;
  const int vo = r * 2048 + c * 16 + head * 256;
  const uint64_t ka = (uint64_t)kc, va = (uint64_t)vc;
  unsigned char* ep = ws + g * 256;
  const unsigned lb = (unsigned)(uintptr_t)(__attribute__((address_space(3))) float*)lds;
  const unsigned po = pay_at(head * 4 + r, (c >> 3) * 4, s) + (c & 7) * 16, pd = pay_at(0, 2, 0) - pay_at(0, 0, 0);
  float o0, o1, o2, o3, o4, o5, o6, o7, nmc, l;
  unsigned epoch;
  asm volatile(
      "v_mov_b32 v31, 0\n"
      "global_load_dword v27, %[bto], %[bt]\n"
      "s_load_dword s66, %[lens], 0x0\n"
      "global_load_dwordx4 v[20:23], %[qo], %[qp]\n"
      "global_load_dword v31, v31, %[ep] sc1\n"
      "s_mov_b32 s40, %[klo]\n"
      "s_mov_b32 s41, %[khi]\n"
      "s_mov_b32 s43, 0x20000\n"
      "s_mov_b32 s44, %[vlo]\n"
      "s_mov_b32 s45, %[vhi]\n"
      "s_mov_b32 s47, 0x20000\n"
      "s_mov_b32 s50, 0x8020000\n"
      "s_mov_b32 s61, 0x05040100\n"
      "s_mov_b32 s62, 0x07060302\n"
      "s_mov_b32 s63, 0x3e0293ee\n"
      "s_waitcnt lgkmcnt(0)\n"
      "s_cmp_lt_i32 s66, 1\n"
      "s_cselect_b32 s67, 1, 0\n"
      "s_cselect_b32 s66, 0x10000, s66\n"
      "s_min_i32 s66, s66, 0x10000\n"
      "s_lshr_b32 s68, s66, 12\n"
      "s_and_b32 s69, s66, 0xfff\n"
      "s_add_u32 s70, s69, 3\n"
      "s_lshr_b32 s70, s70, 2\n"
      "s_mul_i32 s71, %[p0], s70\n"
      "s_add_u32 s72, s71, s70\n"
      "s_lshr_b32 s71, s71, 8\n"
      "s_lshr_b32 s72, s72, 8\n"
      "s_lshr_b32 s73, s71, 2\n"
      "s_lshl_b32 s74, s68, 8\n"
      "s_add_u32 s74, s74, s73\n"
      "s_min_u32 s75, s74, 0xfff\n"
      "s_lshl_b32 s75, s75, 2\n"
      "s_load_dword s64, %[bt], s75\n"
      "s_add_u32 s74, s74, 1\n"
      "s_min_u32 s74, s74, 0xfff\n"
      "s_lshl_b32 s74, s74, 2\n"
      "s_load_dword s65, %[bt], s74\n"
      "s_cmp_lt_u32 s71, s72\n"
      "s_addc_u32 s60, s68, 0\n"
      "v_cmp_gt_u32 vcc, s68, %[jl]\n"
      "s_cmp_lg_u32 s60, 0\n"
      "s_cselect_b32 s49, s50, 0\n"
      "s_waitcnt vmcnt(2)\n"
      "v_lshl_add_u32 v27, v27, 15, %[iu]\n"
      "v_cndmask_b32 v27, 1, v27, vcc\n"
      "s_cmp_lg_u32 s68, 0\n"
      "s_cbranch_scc1 2f\n"
      "1:\n"
      "s_waitcnt lgkmcnt(0)\n"
      "s_cmp_lt_u32 s71, s72\n"
      "s_cbranch_scc0 7f\n"
      "s_lshl_b32 s74, s68, 2\n"
      "s_min_u32 s51, s68, 4\n"
      "s_lshl_b32 s51, s51, 2\n"
      "v_readlane_b32 s48, v27, s51\n"
      "s_add_u32 s75, s71, 0\n"
      "s_lshr_b32 s76, s75, 2\n"
      "s_cmp_eq_u32 s76, s73\n"
      "s_cselect_b32 s76, s64, s65\n"
      "s_lshl_b32 s76, s76, 15\n"
      "s_and_b32 s77, s75, 3\n"
      "s_lshl_b32 s77, s77, 13\n"
      "s_or_b32 s76, s76, s77\n"
      "s_lshl_b32 s77, s75, 2\n"
      "s_sub_u32 s77, s69, s77\n"
      "s_cmp_lt_u32 s75, s72\n"
      "s_cselect_b32 s76, s76, 1\n"
      "s_cselect_b32 s56, s77, 0\n"
      "s_add_u32 m0, s74, 0\n"
      "s_nop 0\n"
      "v_writelane_b32 v27, s48, m0\n"
      "s_mov_b32 m0, s51\n"
      "s_add_u32 s51, s51, 1\n"
      "v_writelane_b32 v27, s76, m0\n"
      "v_readlane_b32 s48, v27, s51\n"
      "s_add_u32 s75, s71, 1\n"
      "s_lshr_b32 s76, s75, 2\n"
      "s_cmp_eq_u32 s76, s73\n"
      "s_cselect_b32 s76, s64, s65\n"
      "s_lshl_b32 s76, s76, 15\n"
      "s_and_b32 s77, s75, 3\n"
      "s_lshl_b32 s77, s77, 13\n"
      "s_or_b32 s76, s76, s77\n"
      "s_lshl_b32 s77, s75, 2\n"
      "s_sub_u32 s77, s69, s77\n"
      "s_cmp_lt_u32 s75, s72\n"
      "s_cselect_b32 s76, s76, 1\n"
      "s_cselect_b32 s57, s77, 0\n"
      "s_add_u32 m0, s74, 1\n"
      "s_nop 0\n"
      "v_writelane_b32 v27, s48, m0\n"
      "s_mov_b32 m0, s51\n"
      "s_add_u32 s51, s51, 1\n"
      "v_writelane_b32 v27, s76, m0\n"
      "v_readlane_b32 s48, v27, s51\n"
      "s_add_u32 s75, s71, 2\n"
      "s_lshr_b32 s76, s75, 2\n"
      "s_cmp_eq_u32 s76, s73\n"
      "s_cselect_b32 s76, s64, s65\n"
      "s_lshl_b32 s76, s76, 15\n"
      "s_and_b32 s77, s75, 3\n"
      "s_lshl_b32 s77, s77, 13\n"
      "s_or_b32 s76, s76, s77\n"
      "s_lshl_b32 s77, s75, 2\n"
      "s_sub_u32 s77, s69, s77\n"
      "s_cmp_lt_u32 s75, s72\n"
      "s_cselect_b32 s76, s76, 1\n"
      "s_cselect_b32 s58, s77, 0\n"
      "s_add_u32 m0, s74, 2\n"
      "s_nop 0\n"
      "v_writelane_b32 v27, s48, m0\n"
      "s_mov_b32 m0, s51\n"
      "s_add_u32 s51, s51, 1\n"
      "v_writelane_b32 v27, s76, m0\n"
      "v_readlane_b32 s48, v27, s51\n"
      "s_add_u32 s75, s71, 3\n"
      "s_lshr_b32 s76, s75, 2\n"
      "s_cmp_eq_u32 s76, s73\n"
      "s_cselect_b32 s76, s64, s65\n"
      "s_lshl_b32 s76, s76, 15\n"
      "s_and_b32 s77, s75, 3\n"
      "s_lshl_b32 s77, s77, 13\n"
      "s_or_b32 s76, s76, s77\n"
      "s_lshl_b32 s77, s75, 2\n"
      "s_sub_u32 s77, s69, s77\n"
      "s_cmp_lt_u32 s75, s72\n"
      "s_cselect_b32 s76, s76, 1\n"
      "s_cselect_b32 s59, s77, 0\n"
      "s_add_u32 m0, s74, 3\n"
      "s_nop 0\n"
      "v_writelane_b32 v27, s48, m0\n"
      "s_mov_b32 m0, s51\n"
      "s_add_u32 s51, s51, 1\n"
      "v_writelane_b32 v27, s76, m0\n"
      "7:\n"
      "s_cmp_lg_u32 s68, 0\n"
      "s_cbranch_scc1 3f\n"
      "2:\n"
      "s_nop 0\n"
      "v_readlane_b32 s52, v27, 0\n"
      "v_readlane_b32 s53, v27, 1\n"
      "v_readlane_b32 s54, v27, 2\n"
      "v_readlane_b32 s55, v27, 3\n"
      "s_nop 2\n"
      "s_bitcmp1_b32 s52, 0\n"
      "s_cselect_b32 s42, 0, s49\n"
      "buffer_load_dwordx4 v[32:35], %[ko], s[40:43], s52 offen sc0 sc1 nt\n"
      "s_bitcmp1_b32 s53, 0\n"
      "s_cselect_b32 s42, 0, s49\n"
      "buffer_load_dwordx4 v[36:39], %[ko], s[40:43], s53 offen sc0 sc1 nt\n"
      "s_bitcmp1_b32 s54, 0\n"
      "s_cselect_b32 s42, 0, s49\n"
      "buffer_load_dwordx4 v[40:43], %[ko], s[40:43], s54 offen sc0 sc1 nt\n"
      "s_bitcmp1_b32 s55, 0\n"
      "s_cselect_b32 s42, 0, s49\n"
      "buffer_load_dwordx4 v[44:47], %[ko], s[40:43], s55 offen sc0 sc1 nt\n"
      "s_bitcmp1_b32 s52, 0\n"
      "s_cselect_b32 s46, 0, s49\n"
      "buffer_load_dwordx4 v[48:51], %[vo], s[44:47], s52 offen sc0 sc1 nt\n"
      "s_bitcmp1_b32 s53, 0\n"
      "s_cselect_b32 s46, 0, s49\n"
      "buffer_load_dwordx4 v[52:55], %[vo], s[44:47], s53 offen sc0 sc1 nt\n"
      "s_bitcmp1_b32 s54, 0\n"
      "s_cselect_b32 s46, 0, s49\n"
      "buffer_load_dwordx4 v[56:59], %[vo], s[44:47], s54 offen sc0 sc1 nt\n"
      "s_bitcmp1_b32 s55, 0\n"
      "s_cselect_b32 s46, 0, s49\n"
      "buffer_load_dwordx4 v[60:63], %[vo], s[44:47], s55 offen sc0 sc1 nt\n"
      "s_cmp_lg_u32 s68, 0\n"
      "s_cbranch_scc1 1b\n"
      "3:\n"
      "v_mov_b32 v64, 0\n"
      "ds_write_b32 %[ca], v64\n"
      "v_mov_b32 v96, 0\n"
      "v_mov_b32 v97, 0\n"
      "v_mov_b32 v98, 0\n"
      "v_mov_b32 v99, 0\n"
      "v_mov_b32 v100, 0\n"
      "v_mov_b32 v101, 0\n"
      "v_mov_b32 v102, 0\n"
      "v_mov_b32 v103, 0\n"
      "v_mov_b32 v104, 0\n"
      "v_mov_b32 v105, 0\n"
      "v_mov_b32 v106, 0\n"
      "v_mov_b32 v107, 0\n"
      "v_mov_b32 v108, 0\n"
      "v_mov_b32 v109, 0\n"
      "v_mov_b32 v110, 0\n"
      "v_mov_b32 v111, 0\n"
      "v_mov_b32 v112, 0\n"
      "v_mov_b32 v113, 0\n"
      "v_mov_b32 v114, 0\n"
      "v_mov_b32 v115, 0\n"
      "v_mov_b32 v116, 0\n"
      "v_mov_b32 v117, 0\n"
      "v_mov_b32 v118, 0\n"
      "v_mov_b32 v119, 0\n"
      "v_mov_b32 v120, 0\n"
      "v_mov_b32 v121, 0\n"
      "v_mov_b32 v122, 0\n"
      "v_mov_b32 v123, 0\n"
      "v_mov_b32 v124, 0\n"
      "v_mov_b32 v125, 0\n"
      "v_mov_b32 v126, 0\n"
      "v_mov_b32 v127, 0\n"
      "v_mov_b32 v28, 0xff800000\n"
      "v_mov_b32 v29, 0x7f800000\n"
      "v_mov_b32 v30, 0\n"
      "s_mov_b32 s70, s60\n"
      "s_sub_u32 s51, s60, 2\n"
      "s_cmp_ge_u32 s68, 6\n"
      "s_cselect_b32 s70, s51, s60\n"
      "s_min_u32 s51, s68, 4\n"
      "s_cmp_lt_u32 s71, s72\n"
      "s_cselect_b32 s68, s51, s68\n"
      "s_add_u32 s68, s68, 1\n"
      "s_mov_b32 s48, 0\n"
      "s_cmp_eq_u32 s67, 0\n"
      "s_cbranch_scc1 8f\n"
      "s_waitcnt vmcnt(8)\n"
      "v_mov_b32 v20, 0\n"
      "v_mov_b32 v21, 0\n"
      "v_mov_b32 v22, 0\n"
      "v_mov_b32 v23, 0\n"
      "8:\n"
      "s_waitcnt lgkmcnt(0)\n"
      "s_barrier\n"
      "s_cmp_eq_u32 s60, 0\n"
      "s_cbranch_scc1 6f\n"
      "4:\n"
      "s_add_u32 s48, s48, 1\n"
      "s_cmp_lt_u32 s48, s60\n"
      "s_cselect_b32 s49, s50, 0\n"
      "s_lshl_b32 s51, s48, 2\n"
      "v_readlane_b32 s52, v27, s51\n"
      "s_add_u32 s51, s51, 1\n"
      "v_readlane_b32 s53, v27, s51\n"
      "s_add_u32 s51, s51, 1\n"
      "v_readlane_b32 s54, v27, s51\n"
      "s_add_u32 s51, s51, 1\n"
      "v_readlane_b32 s55, v27, s51\n"
      "s_waitcnt vmcnt(4)\n"
      "v_mfma_f32_16x16x32_bf16 v[64:67], v[32:35], v[20:23], 0\n"
      "v_mfma_f32_16x16x32_bf16 v[68:71], v[36:39], v[20:23], 0\n"
      "v_mfma_f32_16x16x32_bf16 v[72:75], v[40:43], v[20:23], 0\n"
      "v_mfma_f32_16x16x32_bf16 v[76:79], v[44:47], v[20:23], 0\n"
      "s_bitcmp1_b32 s52, 0\n"
      "s_cselect_b32 s42, 0, s49\n"
      "buffer_load_dwordx4 v[32:35], %[ko], s[40:43], s52 offen sc0 sc1 nt\n"
      "s_bitcmp1_b32 s53, 0\n"
      "s_cselect_b32 s42, 0, s49\n"
      "buffer_load_dwordx4 v[36:39], %[ko], s[40:43], s53 offen sc0 sc1 nt\n"
      "s_bitcmp1_b32 s54, 0\n"
      "s_cselect_b32 s42, 0, s49\n"
      "buffer_load_dwordx4 v[40:43], %[ko], s[40:43], s54 offen sc0 sc1 nt\n"
      "s_bitcmp1_b32 s55, 0\n"
      "s_cselect_b32 s42, 0, s49\n"
      "buffer_load_dwordx4 v[44:47], %[ko], s[40:43], s55 offen sc0 sc1 nt\n"
      "v_mov_b32_dpp v64, v64 quad_perm:[0,0,0,0] row_mask:0xf bank_mask:0xf\n"
      "v_mov_b32_dpp v68, v68 quad_perm:[0,0,0,0] row_mask:0xf bank_mask:0xf\n"
      "v_mov_b32_dpp v72, v72 quad_perm:[0,0,0,0] row_mask:0xf bank_mask:0xf\n"
      "v_mov_b32_dpp v76, v76 quad_perm:[0,0,0,0] row_mask:0xf bank_mask:0xf\n"
      "v_add_f32_dpp v64, v65, v64 quad_perm:[1,1,1,1] row_mask:0xf bank_mask:0xf\n"
      "v_add_f32_dpp v68, v69, v68 quad_perm:[1,1,1,1] row_mask:0xf bank_mask:0xf\n"
      "v_add_f32_dpp v72, v73, v72 quad_perm:[1,1,1,1] row_mask:0xf bank_mask:0xf\n"
      "v_add_f32_dpp v76, v77, v76 quad_perm:[1,1,1,1] row_mask:0xf bank_mask:0xf\n"
      "v_add_f32_dpp v64, v66, v64 quad_perm:[2,2,2,2] row_mask:0xf bank_mask:0xf\n"
      "v_add_f32_dpp v68, v70, v68 quad_perm:[2,2,2,2] row_mask:0xf bank_mask:0xf\n"
      "v_add_f32_dpp v72, v74, v72 quad_perm:[2,2,2,2] row_mask:0xf bank_mask:0xf\n"
      "v_add_f32_dpp v76, v78, v76 quad_perm:[2,2,2,2] row_mask:0xf bank_mask:0xf\n"
      "v_add_f32_dpp v64, v67, v64 quad_perm:[3,3,3,3] row_mask:0xf bank_mask:0xf\n"
      "v_add_f32_dpp v68, v71, v68 quad_perm:[3,3,3,3] row_mask:0xf bank_mask:0xf\n"
      "v_add_f32_dpp v72, v75, v72 quad_perm:[3,3,3,3] row_mask:0xf bank_mask:0xf\n"
      "v_add_f32_dpp v76, v79, v76 quad_perm:[3,3,3,3] row_mask:0xf bank_mask:0xf\n"
      "s_cmp_eq_u32 s48, s68\n"
      "s_cbranch_scc1 10f\n"
      "11:\n"
      "v_max3_f32 v65, v64, v68, v72\n"
      "v_max_f32 v65, v65, v76\n"
      "v_cmp_gt_f32 vcc, v65, v28\n"
      "s_cmp_lg_u64 vcc, 0\n"
      "s_cbranch_scc1 12f\n"
      "13:\n"
      "v_fma_f32 v65, v64, s63, v29\n"
      "v_fma_f32 v69, v68, s63, v29\n"
      "v_fma_f32 v73, v72, s63, v29\n"
      "v_fma_f32 v77, v76, s63, v29\n"
      "v_exp_f32 v65, v65\n"
      "v_exp_f32 v69, v69\n"
      "v_exp_f32 v73, v73\n"
      "v_exp_f32 v77, v77\n"
      "v_cvt_pk_bf16_f32 v66, v65, v69\n"
      "v_add_f32 v64, v65, v69\n"
      "v_cvt_pk_bf16_f32 v67, v73, v77\n"
      "v_add_f32 v68, v73, v77\n"
      "v_add_f32 v30, v30, v64\n"
      "v_add_f32 v30, v30, v68\n"
      "s_waitcnt vmcnt(4)\n"
      "v_perm_b32 v80, v52, v48, s61\n"
      "v_perm_b32 v81, v60, v56, s61\n"
      "v_perm_b32 v82, v52, v48, s62\n"
      "v_perm_b32 v83, v60, v56, s62\n"
      "v_perm_b32 v84, v53, v49, s61\n"
      "v_perm_b32 v85, v61, v57, s61\n"
      "v_perm_b32 v86, v53, v49, s62\n"
      "v_perm_b32 v87, v61, v57, s62\n"
      "v_perm_b32 v88, v54, v50, s61\n"
      "v_perm_b32 v89, v62, v58, s61\n"
      "v_perm_b32 v90, v54, v50, s62\n"
      "v_perm_b32 v91, v62, v58, s62\n"
      "v_perm_b32 v92, v55, v51, s61\n"
      "v_perm_b32 v93, v63, v59, s61\n"
      "v_perm_b32 v94, v55, v51, s62\n"
      "v_perm_b32 v95, v63, v59, s62\n"
      "s_bitcmp1_b32 s52, 0\n"
      "s_cselect_b32 s46, 0, s49\n"
      "buffer_load_dwordx4 v[48:51], %[vo], s[44:47], s52 offen sc0 sc1 nt\n"
      "s_bitcmp1_b32 s53, 0\n"
      "s_cselect_b32 s46, 0, s49\n"
      "buffer_load_dwordx4 v[52:55], %[vo], s[44:47], s53 offen sc0 sc1 nt\n"
      "s_bitcmp1_b32 s54, 0\n"
      "s_cselect_b32 s46, 0, s49\n"
      "buffer_load_dwordx4 v[56:59], %[vo], s[44:47], s54 offen sc0 sc1 nt\n"
      "s_bitcmp1_b32 s55, 0\n"
      "s_cselect_b32 s46, 0, s49\n"
      "buffer_load_dwordx4 v[60:63], %[vo], s[44:47], s55 offen sc0 sc1 nt\n"
      "v_mfma_f32_16x16x16_bf16 v[96:99], v[66:67], v[80:81], v[96:99]\n"
      "v_mfma_f32_16x16x16_bf16 v[100:103], v[66:67], v[82:83], v[100:103]\n"
      "v_mfma_f32_16x16x16_bf16 v[104:107], v[66:67], v[84:85], v[104:107]\n"
      "v_mfma_f32_16x16x16_bf16 v[108:111], v[66:67], v[86:87], v[108:111]\n"
      "v_mfma_f32_16x16x16_bf16 v[112:115], v[66:67], v[88:89], v[112:115]\n"
      "v_mfma_f32_16x16x16_bf16 v[116:119], v[66:67], v[90:91], v[116:119]\n"
      "v_mfma_f32_16x16x16_bf16 v[120:123], v[66:67], v[92:93], v[120:123]\n"
      "v_mfma_f32_16x16x16_bf16 v[124:127], v[66:67], v[94:95], v[124:127]\n"
      "s_cmp_lt_u32 s48, s70\n"
      "s_cbranch_scc1 4b\n"
      "s_cmp_eq_u32 s70, s60\n"
      "s_cbranch_scc1 6f\n"
      "s_lshl_b32 s51, s48, 2\n"
      "s_add_u32 s51, s51, 4\n"
      "v_readlane_b32 s52, v27, s51\n"
      "s_add_u32 s51, s51, 1\n"
      "v_readlane_b32 s53, v27, s51\n"
      "s_add_u32 s51, s51, 1\n"
      "v_readlane_b32 s54, v27, s51\n"
      "s_add_u32 s51, s51, 1\n"
      "v_readlane_b32 s55, v27, s51\n"
      "s_add_u32 s51, s51, 1\n"
      "s_mov_b32 s42, s50\n"
      "s_mov_b32 s46, s50\n"
      "s_waitcnt vmcnt(4)\n"
      "v_mfma_f32_16x16x32_bf16 v[64:67], v[32:35], v[20:23], 0\n"
      "v_mfma_f32_16x16x32_bf16 v[68:71], v[36:39], v[20:23], 0\n"
      "v_mfma_f32_16x16x32_bf16 v[72:75], v[40:43], v[20:23], 0\n"
      "v_mfma_f32_16x16x32_bf16 v[76:79], v[44:47], v[20:23], 0\n"
      "buffer_load_dwordx4 v[32:35], %[ko], s[40:43], s52 offen sc0 sc1 nt\n"
      "buffer_load_dwordx4 v[36:39], %[ko], s[40:43], s53 offen sc0 sc1 nt\n"
      "buffer_load_dwordx4 v[40:43], %[ko], s[40:43], s54 offen sc0 sc1 nt\n"
      "buffer_load_dwordx4 v[44:47], %[ko], s[40:43], s55 offen sc0 sc1 nt\n"
      "buffer_load_dwordx4 v[128:131], %[vo], s[44:47], s52 offen sc0 sc1 nt\n"
      "buffer_load_dwordx4 v[132:135], %[vo], s[44:47], s53 offen sc0 sc1 nt\n"
      "buffer_load_dwordx4 v[136:139], %[vo], s[44:47], s54 offen sc0 sc1 nt\n"
      "buffer_load_dwordx4 v[140:143], %[vo], s[44:47], s55 offen sc0 sc1 nt\n"
      "v_mov_b32_dpp v64, v64 quad_perm:[0,0,0,0] row_mask:0xf bank_mask:0xf\n"
      "v_mov_b32_dpp v68, v68 quad_perm:[0,0,0,0] row_mask:0xf bank_mask:0xf\n"
      "v_mov_b32_dpp v72, v72 quad_perm:[0,0,0,0] row_mask:0xf bank_mask:0xf\n"
      "v_mov_b32_dpp v76, v76 quad_perm:[0,0,0,0] row_mask:0xf bank_mask:0xf\n"
      "v_add_f32_dpp v64, v65, v64 quad_perm:[1,1,1,1] row_mask:0xf bank_mask:0xf\n"
      "v_add_f32_dpp v68, v69, v68 quad_perm:[1,1,1,1] row_mask:0xf bank_mask:0xf\n"
      "v_add_f32_dpp v72, v73, v72 quad_perm:[1,1,1,1] row_mask:0xf bank_mask:0xf\n"
      "v_add_f32_dpp v76, v77, v76 quad_perm:[1,1,1,1] row_mask:0xf bank_mask:0xf\n"
      "v_add_f32_dpp v64, v66, v64 quad_perm:[2,2,2,2] row_mask:0xf bank_mask:0xf\n"
      "v_add_f32_dpp v68, v70, v68 quad_perm:[2,2,2,2] row_mask:0xf bank_mask:0xf\n"
      "v_add_f32_dpp v72, v74, v72 quad_perm:[2,2,2,2] row_mask:0xf bank_mask:0xf\n"
      "v_add_f32_dpp v76, v78, v76 quad_perm:[2,2,2,2] row_mask:0xf bank_mask:0xf\n"
      "v_add_f32_dpp v64, v67, v64 quad_perm:[3,3,3,3] row_mask:0xf bank_mask:0xf\n"
      "v_add_f32_dpp v68, v71, v68 quad_perm:[3,3,3,3] row_mask:0xf bank_mask:0xf\n"
      "v_add_f32_dpp v72, v75, v72 quad_perm:[3,3,3,3] row_mask:0xf bank_mask:0xf\n"
      "v_add_f32_dpp v76, v79, v76 quad_perm:[3,3,3,3] row_mask:0xf bank_mask:0xf\n"
      "v_max3_f32 v65, v64, v68, v72\n"
      "v_max_f32 v65, v65, v76\n"
      "v_cmp_gt_f32 vcc, v65, v28\n"
      "s_cmp_lg_u64 vcc, 0\n"
      "s_cbranch_scc1 14f\n"
      "15:\n"
      "v_fma_f32 v65, v64, s63, v29\n"
      "v_fma_f32 v69, v68, s63, v29\n"
      "v_fma_f32 v73, v72, s63, v29\n"
      "v_fma_f32 v77, v76, s63, v29\n"
      "v_exp_f32 v65, v65\n"
      "v_exp_f32 v69, v69\n"
      "v_exp_f32 v73, v73\n"
      "v_exp_f32 v77, v77\n"
      "v_cvt_pk_bf16_f32 v66, v65, v69\n"
      "v_add_f32 v64, v65, v69\n"
      "v_cvt_pk_bf16_f32 v67, v73, v77\n"
      "v_add_f32 v68, v73, v77\n"
      "v_add_f32 v30, v30, v64\n"
      "v_add_f32 v30, v30, v68\n"
      "s_waitcnt vmcnt(8)\n"
      "v_perm_b32 v80, v52, v48, s61\n"
      "v_perm_b32 v81, v60, v56, s61\n"
      "v_perm_b32 v82, v52, v48, s62\n"
      "v_perm_b32 v83, v60, v56, s62\n"
      "v_perm_b32 v84, v53, v49, s61\n"
      "v_perm_b32 v85, v61, v57, s61\n"
      "v_perm_b32 v86, v53, v49, s62\n"
      "v_perm_b32 v87, v61, v57, s62\n"
      "v_perm_b32 v88, v54, v50, s61\n"
      "v_perm_b32 v89, v62, v58, s61\n"
      "v_perm_b32 v90, v54, v50, s62\n"
      "v_perm_b32 v91, v62, v58, s62\n"
      "v_perm_b32 v92, v55, v51, s61\n"
      "v_perm_b32 v93, v63, v59, s61\n"
      "v_perm_b32 v94, v55, v51, s62\n"
      "v_perm_b32 v95, v63, v59, s62\n"
      "s_nop 1\n"
      "v_mfma_f32_16x16x16_bf16 v[96:99], v[66:67], v[80:81], v[96:99]\n"
      "v_mfma_f32_16x16x16_bf16 v[100:103], v[66:67], v[82:83], v[100:103]\n"
      "v_mfma_f32_16x16x16_bf16 v[104:107], v[66:67], v[84:85], v[104:107]\n"
      "v_mfma_f32_16x16x16_bf16 v[108:111], v[66:67], v[86:87], v[108:111]\n"
      "v_mfma_f32_16x16x16_bf16 v[112:115], v[66:67], v[88:89], v[112:115]\n"
      "v_mfma_f32_16x16x16_bf16 v[116:119], v[66:67], v[90:91], v[116:119]\n"
      "v_mfma_f32_16x16x16_bf16 v[120:123], v[66:67], v[92:93], v[120:123]\n"
      "v_mfma_f32_16x16x16_bf16 v[124:127], v[66:67], v[94:95], v[124:127]\n"
      "s_waitcnt vmcnt(4)\n"
      "v_mfma_f32_16x16x32_bf16 v[64:67], v[32:35], v[20:23], 0\n"
      "v_mfma_f32_16x16x32_bf16 v[68:71], v[36:39], v[20:23], 0\n"
      "v_mfma_f32_16x16x32_bf16 v[72:75], v[40:43], v[20:23], 0\n"
      "v_mfma_f32_16x16x32_bf16 v[76:79], v[44:47], v[20:23], 0\n"
      "s_nop 7\n"
      "v_mov_b32_dpp v64, v64 quad_perm:[0,0,0,0] row_mask:0xf bank_mask:0xf\n"
      "v_mov_b32_dpp v68, v68 quad_perm:[0,0,0,0] row_mask:0xf bank_mask:0xf\n"
      "v_mov_b32_dpp v72, v72 quad_perm:[0,0,0,0] row_mask:0xf bank_mask:0xf\n"
      "v_mov_b32_dpp v76, v76 quad_perm:[0,0,0,0] row_mask:0xf bank_mask:0xf\n"
      "v_add_f32_dpp v64, v65, v64 quad_perm:[1,1,1,1] row_mask:0xf bank_mask:0xf\n"
      "v_add_f32_dpp v68, v69, v68 quad_perm:[1,1,1,1] row_mask:0xf bank_mask:0xf\n"
      "v_add_f32_dpp v72, v73, v72 quad_perm:[1,1,1,1] row_mask:0xf bank_mask:0xf\n"
      "v_add_f32_dpp v76, v77, v76 quad_perm:[1,1,1,1] row_mask:0xf bank_mask:0xf\n"
      "v_add_f32_dpp v64, v66, v64 quad_perm:[2,2,2,2] row_mask:0xf bank_mask:0xf\n"
      "v_add_f32_dpp v68, v70, v68 quad_perm:[2,2,2,2] row_mask:0xf bank_mask:0xf\n"
      "v_add_f32_dpp v72, v74, v72 quad_perm:[2,2,2,2] row_mask:0xf bank_mask:0xf\n"
      "v_add_f32_dpp v76, v78, v76 quad_perm:[2,2,2,2] row_mask:0xf bank_mask:0xf\n"
      "v_add_f32_dpp v64, v67, v64 quad_perm:[3,3,3,3] row_mask:0xf bank_mask:0xf\n"
      "v_add_f32_dpp v68, v71, v68 quad_perm:[3,3,3,3] row_mask:0xf bank_mask:0xf\n"
      "v_add_f32_dpp v72, v75, v72 quad_perm:[3,3,3,3] row_mask:0xf bank_mask:0xf\n"
      "v_add_f32_dpp v76, v79, v76 quad_perm:[3,3,3,3] row_mask:0xf bank_mask:0xf\n"
      "v_max3_f32 v65, v64, v68, v72\n"
      "v_max_f32 v65, v65, v76\n"
      "v_cmp_gt_f32 vcc, v65, v28\n"
      "s_cmp_lg_u64 vcc, 0\n"
      "s_cbranch_scc1 16f\n"
      "17:\n"
      "v_fma_f32 v65, v64, s63, v29\n"
      "v_fma_f32 v69, v68, s63, v29\n"
      "v_fma_f32 v73, v72, s63, v29\n"
      "v_fma_f32 v77, v76, s63, v29\n"
      "v_exp_f32 v65, v65\n"
      "v_exp_f32 v69, v69\n"
      "v_exp_f32 v73, v73\n"
      "v_exp_f32 v77, v77\n"
      "v_cvt_pk_bf16_f32 v66, v65, v69\n"
      "v_add_f32 v64, v65, v69\n"
      "v_cvt_pk_bf16_f32 v67, v73, v77\n"
      "v_add_f32 v68, v73, v77\n"
      "v_add_f32 v30, v30, v64\n"
      "v_add_f32 v30, v30, v68\n"
      "s_waitcnt vmcnt(0)\n"
      "v_perm_b32 v80, v132, v128, s61\n"
      "v_perm_b32 v81, v140, v136, s61\n"
      "v_perm_b32 v82, v132, v128, s62\n"
      "v_perm_b32 v83, v140, v136, s62\n"
      "v_perm_b32 v84, v133, v129, s61\n"
      "v_perm_b32 v85, v141, v137, s61\n"
      "v_perm_b32 v86, v133, v129, s62\n"
      "v_perm_b32 v87, v141, v137, s62\n"
      "v_perm_b32 v88, v134, v130, s61\n"
      "v_perm_b32 v89, v142, v138, s61\n"
      "v_perm_b32 v90, v134, v130, s62\n"
      "v_perm_b32 v91, v142, v138, s62\n"
      "v_perm_b32 v92, v135, v131, s61\n"
      "v_perm_b32 v93, v143, v139, s61\n"
      "v_perm_b32 v94, v135, v131, s62\n"
      "v_perm_b32 v95, v143, v139, s62\n"
      "s_nop 1\n"
      "v_mfma_f32_16x16x16_bf16 v[96:99], v[66:67], v[80:81], v[96:99]\n"
      "v_mfma_f32_16x16x16_bf16 v[100:103], v[66:67], v[82:83], v[100:103]\n"
      "v_mfma_f32_16x16x16_bf16 v[104:107], v[66:67], v[84:85], v[104:107]\n"
      "v_mfma_f32_16x16x16_bf16 v[108:111], v[66:67], v[86:87], v[108:111]\n"
      "v_mfma_f32_16x16x16_bf16 v[112:115], v[66:67], v[88:89], v[112:115]\n"
      "v_mfma_f32_16x16x16_bf16 v[116:119], v[66:67], v[90:91], v[116:119]\n"
      "v_mfma_f32_16x16x16_bf16 v[120:123], v[66:67], v[92:93], v[120:123]\n"
      "v_mfma_f32_16x16x16_bf16 v[124:127], v[66:67], v[94:95], v[124:127]\n"
      "s_branch 6f\n"
      "10:\n"
      "v_mov_b32 v69, 0xff800000\n"
      "v_cmp_gt_u32 vcc, s56, %[rr]\n"
      "s_nop 1\n"
      "v_cndmask_b32 v64, v69, v64, vcc\n"
      "v_cmp_gt_u32 vcc, s57, %[rr]\n"
      "s_nop 1\n"
      "v_cndmask_b32 v68, v69, v68, vcc\n"
      "v_cmp_gt_u32 vcc, s58, %[rr]\n"
      "s_nop 1\n"
      "v_cndmask_b32 v72, v69, v72, vcc\n"
      "v_cmp_gt_u32 vcc, s59, %[rr]\n"
      "s_nop 1\n"
      "v_cndmask_b32 v76, v69, v76, vcc\n"
      "s_branch 11b\n"
      "12:\n"
      "v_mov_b32 v69, v65\n"
      "s_nop 1\n"
      "v_permlane16_swap_b32 v65, v69\n"
      "s_nop 1\n"
      "v_max_f32 v65, v65, v69\n"
      "v_mov_b32 v69, v65\n"
      "s_nop 1\n"
      "v_permlane32_swap_b32 v65, v69\n"
      "s_nop 1\n"
      "v_max3_f32 v28, v65, v69, v28\n"
      "v_mul_f32 v65, s63, v28\n"
      "v_add_f32 v69, v65, v29\n"
      "v_exp_f32_e64 v69, -v69\n"
      "v_sub_f32 v29, 0, v65\n"
      "v_mul_f32 v30, v30, v69\n"
      "s_nop 1\n"
      "v_mov_b32_dpp v73, v69 row_newbcast:0 row_mask:0x1 bank_mask:0xf\n"
      "s_nop 1\n"
      "v_mov_b32_dpp v73, v69 row_newbcast:4 row_mask:0x2 bank_mask:0xf\n"
      "s_nop 1\n"
      "v_mov_b32_dpp v73, v69 row_newbcast:8 row_mask:0x4 bank_mask:0xf\n"
      "s_nop 1\n"
      "v_mov_b32_dpp v73, v69 row_newbcast:12 row_mask:0x8 bank_mask:0xf\n"
      "s_nop 1\n"
      "v_mul_f32 v96, v73, v96\n"
      "v_mul_f32 v100, v73, v100\n"
      "v_mul_f32 v104, v73, v104\n"
      "v_mul_f32 v108, v73, v108\n"
      "v_mul_f32 v112, v73, v112\n"
      "v_mul_f32 v116, v73, v116\n"
      "v_mul_f32 v120, v73, v120\n"
      "v_mul_f32 v124, v73, v124\n"
      "s_branch 13b\n"
      "14:\n"
      "v_mov_b32 v69, v65\n"
      "s_nop 1\n"
      "v_permlane16_swap_b32 v65, v69\n"
      "s_nop 1\n"
      "v_max_f32 v65, v65, v69\n"
      "v_mov_b32 v69, v65\n"
      "s_nop 1\n"
      "v_permlane32_swap_b32 v65, v69\n"
      "s_nop 1\n"
      "v_max3_f32 v28, v65, v69, v28\n"
      "v_mul_f32 v65, s63, v28\n"
      "v_add_f32 v69, v65, v29\n"
      "v_exp_f32_e64 v69, -v69\n"
      "v_sub_f32 v29, 0, v65\n"
      "v_mul_f32 v30, v30, v69\n"
      "s_nop 1\n"
      "v_mov_b32_dpp v73, v69 row_newbcast:0 row_mask:0x1 bank_mask:0xf\n"
      "s_nop 1\n"
      "v_mov_b32_dpp v73, v69 row_newbcast:4 row_mask:0x2 bank_mask:0xf\n"
      "s_nop 1\n"
      "v_mov_b32_dpp v73, v69 row_newbcast:8 row_mask:0x4 bank_mask:0xf\n"
      "s_nop 1\n"
      "v_mov_b32_dpp v73, v69 row_newbcast:12 row_mask:0x8 bank_mask:0xf\n"
      "s_nop 1\n"
      "v_mul_f32 v96, v73, v96\n"
      "v_mul_f32 v100, v73, v100\n"
      "v_mul_f32 v104, v73, v104\n"
      "v_mul_f32 v108, v73, v108\n"
      "v_mul_f32 v112, v73, v112\n"
      "v_mul_f32 v116, v73, v116\n"
      "v_mul_f32 v120, v73, v120\n"
      "v_mul_f32 v124, v73, v124\n"
      "s_branch 15b\n"
      "16:\n"
      "v_mov_b32 v69, v65\n"
      "s_nop 1\n"
      "v_permlane16_swap_b32 v65, v69\n"
      "s_nop 1\n"
      "v_max_f32 v65, v65, v69\n"
      "v_mov_b32 v69, v65\n"
      "s_nop 1\n"
      "v_permlane32_swap_b32 v65, v69\n"
      "s_nop 1\n"
      "v_max3_f32 v28, v65, v69, v28\n"
      "v_mul_f32 v65, s63, v28\n"
      "v_add_f32 v69, v65, v29\n"
      "v_exp_f32_e64 v69, -v69\n"
      "v_sub_f32 v29, 0, v65\n"
      "v_mul_f32 v30, v30, v69\n"
      "s_nop 1\n"
      "v_mov_b32_dpp v73, v69 row_newbcast:0 row_mask:0x1 bank_mask:0xf\n"
      "s_nop 1\n"
      "v_mov_b32_dpp v73, v69 row_newbcast:4 row_mask:0x2 bank_mask:0xf\n"
      "s_nop 1\n"
      "v_mov_b32_dpp v73, v69 row_newbcast:8 row_mask:0x4 bank_mask:0xf\n"
      "s_nop 1\n"
      "v_mov_b32_dpp v73, v69 row_newbcast:12 row_mask:0x8 bank_mask:0xf\n"
      "s_nop 1\n"
      "v_mul_f32 v96, v73, v96\n"
      "v_mul_f32 v100, v73, v100\n"
      "v_mul_f32 v104, v73, v104\n"
      "v_mul_f32 v108, v73, v108\n"
      "v_mul_f32 v112, v73, v112\n"
      "v_mul_f32 v116, v73, v116\n"
      "v_mul_f32 v120, v73, v120\n"
      "v_mul_f32 v124, v73, v124\n"
      "s_branch 17b\n"
      "6:\n"
      "s_waitcnt vmcnt(0) lgkmcnt(0)\n"
      "s_nop 7\n"
      : "={v96}"(o0), "={v100}"(o1), "={v104}"(o2), "={v108}"(o3), "={v112}"(o4), "={v116}"(o5), "={v120}"(o6), "={v124}"(o7),
        "={v29}"(nmc), "={v30}"(l), "={v31}"(epoch)
      : [bto] "{v16}"(bto), [qo] "{v17}"(qo), [ko] "{v18}"(ko), [vo] "{v19}"(vo), [jl] "{v24}"(lane >> 2), [iu] "{v25}"((lane & 3) * 8192), [rr] "{v26}"(r),
        [ca] "{v14}"(lb + L_C + (wave & 3) * 4), [wo] "{v15}"(po + (wave >> 2) * pd), [ws] "s"(ws),
        [bt] "s"(bt), [lens] "s"(lens), [qp] "s"(q), [ep] "s"(ep), [p0] "s"(p0), "s"(out),
        [klo] "s"((unsigned)ka), [khi] "s"((unsigned)(ka >> 32) & 0xffffu), [vlo] "s"((unsigned)va), [vhi] "s"((unsigned)(va >> 32) & 0xffffu)
      :
        "s40", "s41", "s42", "s43", "s44", "s45", "s46", "s47", "s48", "s49", "s50", "s51", "s52", "s53", "s54", "s55", "s56", "s57", "s58", "s59",
        "s60", "s61", "s62", "s63", "s64", "s65", "s66", "s67", "s68", "s69", "s70", "s71", "s72", "s73", "s74", "s75", "s76", "s77", "vcc", "scc",
        "m0", "memory", "v20", "v21", "v22", "v23", "v27", "v28", "v32", "v33", "v34", "v35", "v36", "v37", "v38", "v39", "v40", "v41", "v42", "v43",
        "v44", "v45", "v46", "v47", "v48", "v49", "v50", "v51", "v52", "v53", "v54", "v55", "v56", "v57", "v58", "v59", "v60", "v61", "v62", "v63",
        "v64", "v65", "v66", "v67", "v68", "v69", "v70", "v71", "v72", "v73", "v74", "v75", "v76", "v77", "v78", "v79", "v80", "v81", "v82", "v83",
        "v84", "v85", "v86", "v87", "v88", "v89", "v90", "v91", "v92", "v93", "v94", "v95", "v97", "v98", "v99", "v101", "v102", "v103", "v105",
        "v106", "v107", "v109", "v110", "v111", "v113", "v114", "v115", "v117", "v118", "v119", "v121", "v122", "v123", "v125", "v126", "v127", "v128",
        "v129", "v130", "v131", "v132", "v133", "v134", "v135", "v136", "v137", "v138", "v139", "v140", "v141", "v142", "v143");
  STAMP(1);
  const unsigned tag = (epoch + 1) & 0xffu;
  {
#pragma clang fp contract(off)
    f32x2 p01, p23, p45, p67;
    f32x4 la, lp;
    float ma, mp;
    unsigned old;
    const unsigned ra = lb + wave * 2048 + lane * 4, rp = ra ^ 8192, mw = lb + wave * 16 + (c >> 2) * 4, lw = lb + wave * 64 + (c >> 2) * 16 + r * 4;
    const unsigned mr = lb + wave * 16 + r * 4, lr = lb + wave * 64 + r * 16;
    asm volatile(
        "ds_write2st64_b32 %[ra], %[o0], %[o1] offset0:0 offset1:1\n ds_write2st64_b32 %[ra], %[o2], %[o3] offset0:2 offset1:3\n"
        "ds_write2st64_b32 %[ra], %[o4], %[o5] offset0:4 offset1:5\n ds_write2st64_b32 %[ra], %[o6], %[o7] offset0:6 offset1:7\n"
        "ds_write_b32 %[mw], %[m] offset:%[LM]\n ds_write_b32 %[lw], %[l] offset:%[LL]\n"
        "s_mov_b64 exec, 1\n ds_add_rtn_u32 %[old], %[ca], %[one]\n s_mov_b64 exec, -1\n"
        "ds_read_b32 %[ma], %[mr] offset:%[LM]\n ds_read_b32 %[mp], %[mq] offset:%[LM]\n"
        "ds_read_b128 %[la], %[lr] offset:%[LL]\n ds_read_b128 %[lp], %[lq] offset:%[LL]\n"
        "ds_read2st64_b32 %[p01], %[rp] offset0:0 offset1:1\n ds_read2st64_b32 %[p23], %[rp] offset0:2 offset1:3\n"
        "ds_read2st64_b32 %[p45], %[rp] offset0:4 offset1:5\n ds_read2st64_b32 %[p67], %[rp] offset0:6 offset1:7\n"
        "s_waitcnt lgkmcnt(0)"
        : [old] "=&v"(old), [ma] "=&v"(ma), [mp] "=&v"(mp), [la] "=&v"(la), [lp] "=&v"(lp), [p01] "=&v"(p01), [p23] "=&v"(p23), [p45] "=&v"(p45), [p67] "=&v"(p67)
        : [ra] "v"(ra), [rp] "v"(rp), [mw] "v"(mw), [lw] "v"(lw), [mr] "v"(mr), [mq] "v"(mr ^ 64), [lr] "v"(lr), [lq] "v"(lr ^ 256),
          [ca] "v"(lb + L_C + (wave & 3) * 4), [one] "v"(1u), [m] "v"(fmaxf(-nmc, -3.0e38f)), [l] "v"(l),
          [o0] "v"(o0), [o1] "v"(o1), [o2] "v"(o2), [o3] "v"(o3), [o4] "v"(o4), [o5] "v"(o5), [o6] "v"(o6), [o7] "v"(o7), [LM] "n"(L_M), [LL] "n"(L_L)
        : "memory");
    STAMP(2);
    if (__builtin_amdgcn_readfirstlane(old) != 0) {
      const float M = fmaxf(ma, mp);
      const float wa = __builtin_amdgcn_exp2f(ma - M), wp = __builtin_amdgcn_exp2f(mp - M);
      const float L = wa * ((la[0] + la[1]) + (la[2] + la[3])) + wp * ((lp[0] + lp[1]) + (lp[2] + lp[3]));
      const float inv = L > 0.f ? 1.0f / L : 0.f;
      const float fa = wa * inv, fp = wp * inv;
      const unsigned lse = __float_as_uint(fmaxf(M + __builtin_amdgcn_logf(L), -3.0e38f)) >> (8 * (c & 3));
      const u32x4 s0 = {tagged(fa * o0 + fp * p01[0], tag), tagged(fa * o1 + fp * p01[1], tag), tagged(fa * o2 + fp * p23[0], tag), tagged(fa * o3 + fp * p23[1], lse)};
      const u32x4 s1 = {tagged(fa * o4 + fp * p45[0], tag), tagged(fa * o5 + fp * p45[1], tag), tagged(fa * o6 + fp * p67[0], tag), tagged(fa * o7 + fp * p67[1], lse)};
      asm volatile("global_store_dwordx4 %0, %1, %4 sc1\n global_store_dwordx4 %2, %3, %4 sc1" ::"v"(po), "v"(s0), "v"(po + pd), "v"(s1), "s"(ws) : "memory");
      STAMP(3);
      asm volatile("s_waitcnt vmcnt(0)" ::: "memory");
      STAMP(7);
    }
  }
  __syncthreads();
  const int H = g * 16 + (s >> 3);
  u32x4 pay;
  {
    const unsigned qo2 = pay_at(H, s & 7, tid >> 2) + (tid & 3) * 16, tag3 = tag * 0x010101u;
    unsigned long long want = ~0ull;
    asm volatile("global_load_dwordx4 %0, %1, %2 sc1\n s_waitcnt vmcnt(0)" : "=&v"(pay) : "v"(qo2), "s"(ws) : "memory");
#pragma nounroll
    for (int spin = 0; spin < (1 << 17); ++spin) {
      const unsigned got = __builtin_amdgcn_perm(pay[2], __builtin_amdgcn_perm(pay[1], pay[0], 0x0c0c0400), 0x0c040100);
      unsigned long long b = __builtin_amdgcn_ballot_w64(got != tag3);
      if (b == 0) break;
      b |= b >> 1; b |= b >> 2; b &= 0x1111111111111111ull;
      want = b * 15;
      __builtin_amdgcn_s_sleep(SLEEP);
      asm volatile("s_mov_b64 exec, %3\n global_load_dwordx4 %0, %1, %2 sc1\n s_mov_b64 exec, -1\n s_waitcnt vmcnt(0)" : "+v"(pay) : "v"(qo2), "s"(ws), "s"(want) : "memory");
    }
  }
  STAMP(4);
  float* fin = lds + L_F / 4;
  {
    unsigned lb8 = (pay[3] & 0xffu) << (8 * (lane & 3));
    lb8 |= __builtin_amdgcn_mov_dpp(lb8, 0xb1, 0xf, 0xf, true);
    lb8 |= __builtin_amdgcn_mov_dpp(lb8, 0x4e, 0xf, 0xf, true);
    const float m = __uint_as_float(lb8);
    float M = fmaxf(m, dpp<ROR4>(m)); M = fmaxf(M, dpp<ROR8>(M)); M = xr_max(M);
    float w = __builtin_amdgcn_exp2f(m - M);
    pay = (pay & 0xffffff00u) | 0x80u;
    const auto x = __builtin_amdgcn_permlane32_swap(__float_as_uint(w * __uint_as_float(pay[0])), __float_as_uint(w * __uint_as_float(pay[2])), false, false);
    const auto y = __builtin_amdgcn_permlane32_swap(__float_as_uint(w * __uint_as_float(pay[1])), __float_as_uint(w * __uint_as_float(pay[3])), false, false);
    const auto z = __builtin_amdgcn_permlane16_swap(__float_as_uint(__uint_as_float(x[0]) + __uint_as_float(x[1])), __float_as_uint(__uint_as_float(y[0]) + __uint_as_float(y[1])), false, false);
    float v = __uint_as_float(z[0]) + __uint_as_float(z[1]);
    v += dpp<ROR4>(v); v += dpp<ROR8>(v);
    w += dpp<ROR4>(w); w += dpp<ROR8>(w); w = xr_sum(w);
    if (c < 4) fin[wave * 20 + 4 + c * 4 + r] = v;
    if (lane == 0) *reinterpret_cast<f32x2*>(fin + wave * 20) = f32x2{M, w};
  }
  __syncthreads();
  STAMP(5);
  if (wave == 0) {
    const int w8 = lane >> 3, dp = lane & 7;
    const f32x2 a = *reinterpret_cast<const f32x2*>(fin + w8 * 20), o = *reinterpret_cast<const f32x2*>(fin + w8 * 20 + 4 + dp * 2);
    float M = fmaxf(a[0], dpp<ROR8>(a[0])); M = xr_max(M);
    const float w = __builtin_amdgcn_exp2f(a[0] - M);
    float v[3] = {w * o[0], w * o[1], w * a[1]};
#pragma unroll
    for (int i = 0; i < 3; ++i) { v[i] += dpp<ROR8>(v[i]); v[i] = xr_sum(v[i]); }
    const float inv = 1.0f / v[2];
    const bf16x2 res = {(__bf16)(v[0] * inv), (__bf16)(v[1] * inv)};
    if (lane < 8) out[H * 64 + (s & 4) * 8 + (s & 1) * 16 + (dp >> 1) * 4 + (s & 2) + (dp & 1)] = __builtin_bit_cast(unsigned, res);
    if (s == 0 && lane == 0) __hip_atomic_store(reinterpret_cast<unsigned*>(ep), epoch + 1, __ATOMIC_RELAXED, __HIP_MEMORY_SCOPE_AGENT);
  }
}
}

extern "C" void kernel_launch(void* const* d_in, const int* in_sizes, int n_in, void* d_out, int out_size, void* d_ws, size_t ws_size,
                              hipStream_t stream) {
  attn_fused<<<GRID, WAVES * 64, 0, stream>>>((const unsigned char*)d_in[0], (const unsigned char*)d_in[1], (const unsigned char*)d_in[2],
                                              (const int*)d_in[3], (const int*)d_in[4], (unsigned*)d_out, (unsigned char*)d_ws);
}
```

```cpp
#include <hip/hip_runtime.h>
#include <hip/hip_bf16.h>
#include <stdint.h>

namespace {
constexpr int WAVES = 8, GRID = 256;

__global__ void __launch_bounds__(WAVES * 64, 2)
attn_fused(const unsigned char* __restrict__ q, const unsigned char* __restrict__ kc, const unsigned char* __restrict__ vc,
           const int* __restrict__ bt, const int* __restrict__ lens, unsigned* __restrict__ out, unsigned char* __restrict__ ws) {
  __shared__ __attribute__((aligned(16))) float lds[18112 / 4];
  asm volatile(
      "s_load_dwordx8 s[4:11], %[ka], 0x0\n"
      "s_load_dwordx4 s[12:15], %[ka], 0x20\n"
      "s_load_dwordx2 s[16:17], %[ka], 0x30\n"
      "s_getpc_b64 s[34:35]\n"
      "v_lshlrev_b32 v3, 4, %[tid]\n"
      "v_min_u32 v3, 5504, v3\n"
      "global_load_dword v4, v3, s[34:35]\n"
      "v_readfirstlane_b32 s3, %[tid]\n"
      "s_and_b32 s18, %[bid], 7\n"
      "s_lshr_b32 s19, %[bid], 4\n"
      "s_lshl3_add_u32 s18, s19, s18\n"
      "s_lshr_b32 s20, s3, 8\n"
      "s_lshl1_add_u32 s20, s18, s20\n"
      "v_bfe_u32 v24, %[tid], 2, 4\n"
      "s_lshl_b32 s21, s20, 2\n"
      "v_lshl_add_u32 v16, v24, 10, s21\n"
      "s_bfe_u32 s22, %[bid], 0x10003\n"
      "s_lshr_b32 s3, s3, 6\n"
      "s_and_b32 s23, s3, 3\n"
      "s_lshl2_add_u32 s23, s22, s23\n"
      "v_bfe_u32 v26, %[tid], 4, 2\n"
      "v_and_b32 v25, 3, %[tid]\n"
      "v_lshlrev_b32 v1, 4, v25\n"
      "v_lshl_add_u32 v1, v26, 6, v1\n"
      "v_and_b32 v2, 3, v24\n"
      "s_lshl_b32 s24, s23, 10\n"
      "s_lshl_b32 s25, s23, 8\n"
      "v_lshl_add_u32 v17, v2, 8, v1\n"
      "v_add_u32 v17, s24, v17\n"
      "v_lshl_add_u32 v18, v2, 11, v1\n"
      "v_add_u32 v18, s25, v18\n"
      "v_and_b32 v1, 15, %[tid]\n"
      "v_lshl_add_u32 v1, v1, 4, s25\n"
      "v_lshl_add_u32 v19, v26, 11, v1\n"
      "v_lshlrev_b32 v25, 13, v25\n"
      "s_lshl_b32 s26, s22, 8\n"
      "v_mov_b32 v31, s26\n"
      "s_lshl_b32 s30, s22, 11\n"
      "s_add_u32 s30, s30, 0x80000\n"
      "v_mov_b32 v10, s30\n"
      "s_waitcnt lgkmcnt(0)\n"
      "s_load_dword s51, s[10:11], s21\n"
      "global_load_dword v27, v16, s[10:11]\n"
      "s_load_dword s66, s[12:13], 0x0\n"
      "global_load_dwordx4 v[20:23], v17, s[4:5]\n"
      "global_load_dword v31, v31, s[16:17] sc1\n"
      "global_load_dword v15, v10, s[16:17] sc1\n"
      "s_mov_b32 s40, s6\n"
      "s_and_b32 s41, s7, 0xffff\n"
      "s_mov_b32 s43, 0x20000\n"
      "s_mov_b32 s44, s8\n"
      "s_and_b32 s45, s9, 0xffff\n"
      "s_mov_b32 s47, 0x20000\n"
      "s_and_b32 s29, s21, 12\n"
      "s_lshr_b32 s27, 0x1210, s29\n"
      "s_and_b32 s27, s27, 3\n"
      "s_lshr_b32 s28, 0x0121, s29\n"
      "s_and_b32 s28, s28, 3\n"
      "s_mov_b32 s50, 0x8020000\n"
      "s_mov_b32 s61, 0x05040100\n"
      "s_mov_b32 s62, 0x07060302\n"
      "s_mov_b32 s63, 0x3e0293ee\n"
      "s_mov_b32 s42, s50\n"
      "s_mov_b32 s46, s50\n"
      "s_waitcnt lgkmcnt(0)\n"
      "s_lshl_b32 s52, s51, 15\n"
      "s_add_u32 s53, s52, 8192\n"
      "s_add_u32 s54, s52, 16384\n"
      "s_add_u32 s55, s52, 24576\n"
      "buffer_load_dwordx4 v[32:35], v18, s[40:43], s52 offen sc0 sc1 nt\n"
      "buffer_load_dwordx4 v[36:39], v18, s[40:43], s53 offen sc0 sc1 nt\n"
      "buffer_load_dwordx4 v[40:43], v18, s[40:43], s54 offen sc0 sc1 nt\n"
      "buffer_load_dwordx4 v[44:47], v18, s[40:43], s55 offen sc0 sc1 nt\n"
      "buffer_load_dwordx4 v[48:51], v19, s[44:47], s52 offen sc0 sc1 nt\n"
      "buffer_load_dwordx4 v[52:55], v19, s[44:47], s53 offen sc0 sc1 nt\n"
      "buffer_load_dwordx4 v[56:59], v19, s[44:47], s54 offen sc0 sc1 nt\n"
      "buffer_load_dwordx4 v[60:63], v19, s[44:47], s55 offen sc0 sc1 nt\n"
      "s_waitcnt vmcnt(11)\n"
      "v_lshl_add_u32 v27, v27, 15, v25\n"
      "s_waitcnt lgkmcnt(0)\n"
      "s_cmp_lt_i32 s66, 1\n"
      "s_cselect_b32 s67, 1, 0\n"
      "s_cselect_b32 s66, 0x10000, s66\n"
      "s_min_i32 s66, s66, 0x10000\n"
      "s_lshr_b32 s68, s66, 12\n"
      "s_and_b32 s69, s66, 0xfff\n"
      "s_add_u32 s70, s69, 3\n"
      "s_lshr_b32 s70, s70, 2\n"
      "s_mul_i32 s71, s20, s70\n"
      "s_add_u32 s72, s71, s70\n"
      "s_mul_i32 s73, s66, 18\n"
      "s_lshr_b32 s73, s73, 12\n"
      "s_min_u32 s73, s73, s70\n"
      "s_sub_u32 s74, 0x400, s70\n"
      "s_min_u32 s73, s73, s74\n"
      "s_mul_i32 s74, s73, s27\n"
      "s_mul_i32 s75, s73, s28\n"
      "s_add_u32 s71, s71, s74\n"
      "s_add_u32 s72, s72, s75\n"
      "s_lshr_b32 s71, s71, 8\n"
      "s_lshr_b32 s72, s72, 8\n"
      "s_lshr_b32 s73, s71, 2\n"
      "s_lshl_b32 s74, s68, 8\n"
      "s_add_u32 s74, s74, s73\n"
      "s_min_u32 s75, s74, 0xfff\n"
      "s_lshl_b32 s75, s75, 2\n"
      "s_load_dword s64, s[10:11], s75\n"
      "s_add_u32 s74, s74, 1\n"
      "s_min_u32 s74, s74, 0xfff\n"
      "s_lshl_b32 s74, s74, 2\n"
      "s_load_dword s65, s[10:11], s74\n"
      "s_cmp_lt_u32 s71, s72\n"
      "s_addc_u32 s60, s68, 0\n"
      "v_cmp_gt_u32 vcc, s68, v24\n"
      "s_cmp_lg_u32 s60, 0\n"
      "s_cselect_b32 s49, s50, 0\n"
      "v_cndmask_b32 v27, 1, v27, vcc\n"
      "1:\n"
      "s_waitcnt lgkmcnt(0)\n"
      "s_cmp_lt_u32 s71, s72\n"
      "s_cbranch_scc0 7f\n"
      "s_lshl_b32 s74, s68, 2\n"
      "s_min_u32 s51, s68, 4\n"
      "s_lshl_b32 s51, s51, 2\n"
      "v_readlane_b32 s48, v27, s51\n"
      "s_add_u32 s75, s71, 0\n"
      "s_lshr_b32 s76, s75, 2\n"
      "s_cmp_eq_u32 s76, s73\n"
      "s_cselect_b32 s76, s64, s65\n"
      "s_lshl_b32 s76, s76, 15\n"
      "s_and_b32 s77, s75, 3\n"
      "s_lshl_b32 s77, s77, 13\n"
      "s_or_b32 s76, s76, s77\n"
      "s_lshl_b32 s77, s75, 2\n"
      "s_sub_u32 s77, s69, s77\n"
      "s_cmp_lt_u32 s75, s72\n"
      "s_cselect_b32 s76, s76, 1\n"
      "s_cselect_b32 s56, s77, 0\n"
      "s_add_u32 m0, s74, 0\n"
      "s_nop 0\n"
      "v_writelane_b32 v27, s48, m0\n"
      "s_mov_b32 m0, s51\n"
      "s_add_u32 s51, s51, 1\n"
      "v_writelane_b32 v27, s76, m0\n"
      "v_readlane_b32 s48, v27, s51\n"
      "s_add_u32 s75, s71, 1\n"
      "s_lshr_b32 s76, s75, 2\n"
      "s_cmp_eq_u32 s76, s73\n"
      "s_cselect_b32 s76, s64, s65\n"
      "s_lshl_b32 s76, s76, 15\n"
      "s_and_b32 s77, s75, 3\n"
      "s_lshl_b32 s77, s77, 13\n"
      "s_or_b32 s76, s76, s77\n"
      "s_lshl_b32 s77, s75, 2\n"
      "s_sub_u32 s77, s69, s77\n"
      "s_cmp_lt_u32 s75, s72\n"
      "s_cselect_b32 s76, s76, 1\n"
      "s_cselect_b32 s57, s77, 0\n"
      "s_add_u32 m0, s74, 1\n"
      "s_nop 0\n"
      "v_writelane_b32 v27, s48, m0\n"
      "s_mov_b32 m0, s51\n"
      "s_add_u32 s51, s51, 1\n"
      "v_writelane_b32 v27, s76, m0\n"
      "v_readlane_b32 s48, v27, s51\n"
      "s_add_u32 s75, s71, 2\n"
      "s_lshr_b32 s76, s75, 2\n"
      "s_cmp_eq_u32 s76, s73\n"
      "s_cselect_b32 s76, s64, s65\n"
      "s_lshl_b32 s76, s76, 15\n"
      "s_and_b32 s77, s75, 3\n"
      "s_lshl_b32 s77, s77, 13\n"
      "s_or_b32 s76, s76, s77\n"
      "s_lshl_b32 s77, s75, 2\n"
      "s_sub_u32 s77, s69, s77\n"
      "s_cmp_lt_u32 s75, s72\n"
      "s_cselect_b32 s76, s76, 1\n"
      "s_cselect_b32 s58, s77, 0\n"
      "s_add_u32 m0, s74, 2\n"
      "s_nop 0\n"
      "v_writelane_b32 v27, s48, m0\n"
      "s_mov_b32 m0, s51\n"
      "s_add_u32 s51, s51, 1\n"
      "v_writelane_b32 v27, s76, m0\n"
      "v_readlane_b32 s48, v27, s51\n"
      "s_add_u32 s75, s71, 3\n"
      "s_lshr_b32 s76, s75, 2\n"
      "s_cmp_eq_u32 s76, s73\n"
      "s_cselect_b32 s76, s64, s65\n"
      "s_lshl_b32 s76, s76, 15\n"
      "s_and_b32 s77, s75, 3\n"
      "s_lshl_b32 s77, s77, 13\n"
      "s_or_b32 s76, s76, s77\n"
      "s_lshl_b32 s77, s75, 2\n"
      "s_sub_u32 s77, s69, s77\n"
      "s_cmp_lt_u32 s75, s72\n"
      "s_cselect_b32 s76, s76, 1\n"
      "s_cselect_b32 s59, s77, 0\n"
      "s_add_u32 m0, s74, 3\n"
      "s_nop 0\n"
      "v_writelane_b32 v27, s48, m0\n"
      "s_mov_b32 m0, s51\n"
      "s_add_u32 s51, s51, 1\n"
      "v_writelane_b32 v27, s76, m0\n"
      "7:\n"
      "s_cmp_lg_u32 s68, 0\n"
      "s_cbranch_scc1 3f\n"
      "2:\n"
      "s_waitcnt vmcnt(0)\n"
      "v_readlane_b32 s52, v27, 0\n"
      "v_readlane_b32 s53, v27, 1\n"
      "v_readlane_b32 s54, v27, 2\n"
      "v_readlane_b32 s55, v27, 3\n"
      "s_nop 2\n"
      "s_bitcmp1_b32 s52, 0\n"
      "s_cselect_b32 s42, 0, s49\n"
      "buffer_load_dwordx4 v[32:35], v18, s[40:43], s52 offen sc0 sc1 nt\n"
      "s_bitcmp1_b32 s53, 0\n"
      "s_cselect_b32 s42, 0, s49\n"
      "buffer_load_dwordx4 v[36:39], v18, s[40:43], s53 offen sc0 sc1 nt\n"
      "s_bitcmp1_b32 s54, 0\n"
      "s_cselect_b32 s42, 0, s49\n"
      "buffer_load_dwordx4 v[40:43], v18, s[40:43], s54 offen sc0 sc1 nt\n"
      "s_bitcmp1_b32 s55, 0\n"
      "s_cselect_b32 s42, 0, s49\n"
      "buffer_load_dwordx4 v[44:47], v18, s[40:43], s55 offen sc0 sc1 nt\n"
      "s_bitcmp1_b32 s52, 0\n"
      "s_cselect_b32 s46, 0, s49\n"
      "buffer_load_dwordx4 v[48:51], v19, s[44:47], s52 offen sc0 sc1 nt\n"
      "s_bitcmp1_b32 s53, 0\n"
      "s_cselect_b32 s46, 0, s49\n"
      "buffer_load_dwordx4 v[52:55], v19, s[44:47], s53 offen sc0 sc1 nt\n"
      "s_bitcmp1_b32 s54, 0\n"
      "s_cselect_b32 s46, 0, s49\n"
      "buffer_load_dwordx4 v[56:59], v19, s[44:47], s54 offen sc0 sc1 nt\n"
      "s_bitcmp1_b32 s55, 0\n"
      "s_cselect_b32 s46, 0, s49\n"
      "buffer_load_dwordx4 v[60:63], v19, s[44:47], s55 offen sc0 sc1 nt\n"
      "3:\n"
      "s_lshl_b32 s29, s23, 18\n"
      "s_lshl_b32 s30, s18, 7\n"
      "s_add_u32 s29, s29, s30\n"
      "s_add_u32 s29, s29, 0x100000\n"
      "s_lshr_b32 s31, s18, 3\n"
      "s_lshl4_add_u32 s31, s22, s31\n"
      "s_bfe_u32 s30, s18, 0x20001\n"
      "s_lshl2_add_u32 s30, s31, s30\n"
      "s_lshl_b32 s30, s30, 14\n"
      "s_and_b32 s48, s18, 1\n"
      "s_lshl_b32 s48, s48, 6\n"
      "s_add_u32 s30, s30, s48\n"
      "s_add_u32 s30, s30, 0x100000\n"
      "s_lshl_b32 s31, s31, 8\n"
      "s_add_u32 s31, s31, s48\n"
      "s_and_b32 s48, s18, 4\n"
      "s_lshl_b32 s48, s48, 5\n"
      "s_add_u32 s31, s31, s48\n"
      "s_and_b32 s48, s18, 2\n"
      "s_lshl2_add_u32 s31, s48, s31\n"
      "s_and_b32 s48, s3, 3\n"
      "s_lshl_b32 s48, s48, 2\n"
      "s_add_u32 s48, s48, 17408\n"
      "v_mov_b32 v181, s48\n"
      "v_mov_b32 v64, 0\n"
      "ds_write_b32 v181, v64\n"
      "v_mov_b32 v11, 17424\n"
      "ds_write2_b32 v11, v64, v64 offset1:1\n"
      "v_mov_b32 v9, 1\n"
      "s_mov_b32 s37, s68\n"
      "s_mov_b32 s36, 0\n"
      "v_and_b32 v64, 63, %[tid]\n"
      "s_lshl_b32 s48, s3, 11\n"
      "v_lshl_add_u32 v176, v64, 2, s48\n"
      "v_xor_b32 v177, 0x2000, v176\n"
      "s_lshl_b32 s48, s3, 7\n"
      "s_add_u32 s48, s48, 16384\n"
      "v_and_b32 v64, 3, v24\n"
      "v_lshl_add_u32 v64, v64, 5, s48\n"
      "v_lshl_add_u32 v178, v26, 2, v64\n"
      "v_lshl_add_u32 v179, v26, 5, s48\n"
      "v_xor_b32 v180, 0x200, v179\n"
      "v_lshrrev_b32 v184, 10, v25\n"
      "v_bfe_u32 v64, %[tid], 3, 1\n"
      "v_lshl_add_u32 v64, v64, 15, s29\n"
      "v_lshl_add_u32 v64, v26, 16, v64\n"
      "v_and_b32 v65, 7, %[tid]\n"
      "v_lshl_add_u32 v182, v65, 4, v64\n"
      "v_add_u32 v183, 0x4000, v182\n"
      "v_lshrrev_b32 v64, 2, %[tid]\n"
      "v_lshl_add_u32 v64, v64, 7, s30\n"
      "v_lshl_add_u32 v185, v184, 1, v64\n"
      "v_xor_b32 v186, 0x2000, v185\n"
      "s_mul_i32 s48, s3, 80\n"
      "s_add_u32 s48, s48, 17472\n"
      "v_lshl_add_u32 v64, v184, 1, s48\n"
      "v_lshl_add_u32 v187, v26, 2, v64\n"
      "v_bfe_u32 v64, %[tid], 3, 3\n"
      "v_mul_u32_u24 v64, 80, v64\n"
      "v_add_u32 v188, 17472, v64\n"
      "v_lshl_add_u32 v189, v65, 3, v188\n"
      "v_bfe_u32 v64, %[tid], 1, 2\n"
      "v_lshl_add_u32 v64, v64, 4, s31\n"
      "v_and_b32 v65, 1, %[tid]\n"
      "v_lshl_add_u32 v190, v65, 2, v64\n"
      "v_mov_b32 v191, 0x80\n"
      "s_mov_b32 s76, 0x07060501\n"
      "s_mov_b32 s77, 0x07060502\n"
      "s_mov_b32 s78, 0xff61b1e6\n"
      "s_mov_b32 s79, 0x07060500\n"
      "s_mov_b32 s80, 0x0c0c0400\n"
      "s_mov_b32 s81, 0x0c040100\n"
      "v_mov_b32 v96, 0\n"
      "v_mov_b32 v97, 0\n"
      "v_mov_b32 v98, 0\n"
      "v_mov_b32 v99, 0\n"
      "v_mov_b32 v100, 0\n"
      "v_mov_b32 v101, 0\n"
      "v_mov_b32 v102, 0\n"
      "v_mov_b32 v103, 0\n"
      "v_mov_b32 v104, 0\n"
      "v_mov_b32 v105, 0\n"
      "v_mov_b32 v106, 0\n"
      "v_mov_b32 v107, 0\n"
      "v_mov_b32 v108, 0\n"
      "v_mov_b32 v109, 0\n"
      "v_mov_b32 v110, 0\n"
      "v_mov_b32 v111, 0\n"
      "v_mov_b32 v112, 0\n"
      "v_mov_b32 v113, 0\n"
      "v_mov_b32 v114, 0\n"
      "v_mov_b32 v115, 0\n"
      "v_mov_b32 v116, 0\n"
      "v_mov_b32 v117, 0\n"
      "v_mov_b32 v118, 0\n"
      "v_mov_b32 v119, 0\n"
      "v_mov_b32 v120, 0\n"
      "v_mov_b32 v121, 0\n"
      "v_mov_b32 v122, 0\n"
      "v_mov_b32 v123, 0\n"
      "v_mov_b32 v124, 0\n"
      "v_mov_b32 v125, 0\n"
      "v_mov_b32 v126, 0\n"
      "v_mov_b32 v127, 0\n"
      "v_mov_b32 v28, 0xff800000\n"
      "v_mov_b32 v29, 0x7f800000\n"
      "v_mov_b32 v30, 0\n"
      "s_mov_b32 s70, s60\n"
      "s_sub_u32 s51, s60, 2\n"
      "s_cmp_ge_u32 s68, 6\n"
      "s_cselect_b32 s70, s51, s60\n"
      "s_min_u32 s51, s68, 4\n"
      "s_cmp_lt_u32 s71, s72\n"
      "s_cselect_b32 s68, s51, s68\n"
      "s_add_u32 s68, s68, 1\n"
      "s_mov_b32 s48, 0\n"
      "s_cmp_eq_u32 s67, 0\n"
      "s_cbranch_scc1 8f\n"
      "s_waitcnt vmcnt(8)\n"
      "v_mov_b32 v20, 0\n"
      "v_mov_b32 v21, 0\n"
      "v_mov_b32 v22, 0\n"
      "v_mov_b32 v23, 0\n"
      "8:\n"
      "s_waitcnt vmcnt(8) lgkmcnt(0)\n"
      "v_readfirstlane_b32 s38, v15\n"
      "s_barrier\n"
      "s_cmp_eq_u32 s60, 0\n"
      "s_cbranch_scc1 6f\n"
      "4:\n"
      "s_add_u32 s48, s48, 1\n"
      "s_cmp_lt_u32 s48, s60\n"
      "s_cselect_b32 s49, s50, 0\n"
      "s_lshl_b32 s51, s48, 2\n"
      "v_readlane_b32 s52, v27, s51\n"
      "s_add_u32 s51, s51, 1\n"
      "v_readlane_b32 s53, v27, s51\n"
      "s_add_u32 s51, s51, 1\n"
      "v_readlane_b32 s54, v27, s51\n"
      "s_add_u32 s51, s51, 1\n"
      "v_readlane_b32 s55, v27, s51\n"
      "s_waitcnt vmcnt(4)\n"
      "v_mfma_f32_16x16x32_bf16 v[64:67], v[32:35], v[20:23], 0\n"
      "v_mfma_f32_16x16x32_bf16 v[68:71], v[36:39], v[20:23], 0\n"
      "v_mfma_f32_16x16x32_bf16 v[72:75], v[40:43], v[20:23], 0\n"
      "v_mfma_f32_16x16x32_bf16 v[76:79], v[44:47], v[20:23], 0\n"
      "s_bitcmp1_b32 s52, 0\n"
      "s_cselect_b32 s42, 0, s49\n"
      "buffer_load_dwordx4 v[32:35], v18, s[40:43], s52 offen sc0 sc1 nt\n"
      "s_bitcmp1_b32 s53, 0\n"
      "s_cselect_b32 s42, 0, s49\n"
      "buffer_load_dwordx4 v[36:39], v18, s[40:43], s53 offen sc0 sc1 nt\n"
      "s_bitcmp1_b32 s54, 0\n"
      "s_cselect_b32 s42, 0, s49\n"
      "buffer_load_dwordx4 v[40:43], v18, s[40:43], s54 offen sc0 sc1 nt\n"
      "s_bitcmp1_b32 s55, 0\n"
      "s_cselect_b32 s42, 0, s49\n"
      "buffer_load_dwordx4 v[44:47], v18, s[40:43], s55 offen sc0 sc1 nt\n"
      "v_mov_b32_dpp v64, v64 quad_perm:[0,0,0,0] row_mask:0xf bank_mask:0xf\n"
      "v_mov_b32_dpp v68, v68 quad_perm:[0,0,0,0] row_mask:0xf bank_mask:0xf\n"
      "v_mov_b32_dpp v72, v72 quad_perm:[0,0,0,0] row_mask:0xf bank_mask:0xf\n"
      "v_mov_b32_dpp v76, v76 quad_perm:[0,0,0,0] row_mask:0xf bank_mask:0xf\n"
      "v_add_f32_dpp v64, v65, v64 quad_perm:[1,1,1,1] row_mask:0xf bank_mask:0xf\n"
      "v_add_f32_dpp v68, v69, v68 quad_perm:[1,1,1,1] row_mask:0xf bank_mask:0xf\n"
      "v_add_f32_dpp v72, v73, v72 quad_perm:[1,1,1,1] row_mask:0xf bank_mask:0xf\n"
      "v_add_f32_dpp v76, v77, v76 quad_perm:[1,1,1,1] row_mask:0xf bank_mask:0xf\n"
      "v_add_f32_dpp v64, v66, v64 quad_perm:[2,2,2,2] row_mask:0xf bank_mask:0xf\n"
      "v_add_f32_dpp v68, v70, v68 quad_perm:[2,2,2,2] row_mask:0xf bank_mask:0xf\n"
      "v_add_f32_dpp v72, v74, v72 quad_perm:[2,2,2,2] row_mask:0xf bank_mask:0xf\n"
      "v_add_f32_dpp v76, v78, v76 quad_perm:[2,2,2,2] row_mask:0xf bank_mask:0xf\n"
      "v_add_f32_dpp v64, v67, v64 quad_perm:[3,3,3,3] row_mask:0xf bank_mask:0xf\n"
      "v_add_f32_dpp v68, v71, v68 quad_perm:[3,3,3,3] row_mask:0xf bank_mask:0xf\n"
      "v_add_f32_dpp v72, v75, v72 quad_perm:[3,3,3,3] row_mask:0xf bank_mask:0xf\n"
      "v_add_f32_dpp v76, v79, v76 quad_perm:[3,3,3,3] row_mask:0xf bank_mask:0xf\n"
      "s_cmp_eq_u32 s48, s68\n"
      "s_cbranch_scc1 10f\n"
      "11:\n"
      "v_max3_f32 v65, v64, v68, v72\n"
      "v_max_f32 v65, v65, v76\n"
      "v_cmp_gt_f32 vcc, v65, v28\n"
      "s_cmp_lg_u64 vcc, 0\n"
      "s_cbranch_scc1 12f\n"
      "13:\n"
      "v_fma_f32 v65, v64, s63, v29\n"
      "v_fma_f32 v69, v68, s63, v29\n"
      "v_fma_f32 v73, v72, s63, v29\n"
      "v_fma_f32 v77, v76, s63, v29\n"
      "v_exp_f32 v65, v65\n"
      "v_exp_f32 v69, v69\n"
      "v_exp_f32 v73, v73\n"
      "v_exp_f32 v77, v77\n"
      "v_cvt_pk_bf16_f32 v66, v65, v69\n"
      "v_add_f32 v64, v65, v69\n"
      "v_cvt_pk_bf16_f32 v67, v73, v77\n"
      "v_add_f32 v68, v73, v77\n"
      "v_add_f32 v30, v30, v64\n"
      "v_add_f32 v30, v30, v68\n"
      "s_waitcnt vmcnt(4)\n"
      "v_perm_b32 v80, v52, v48, s61\n"
      "v_perm_b32 v81, v60, v56, s61\n"
      "v_perm_b32 v82, v52, v48, s62\n"
      "v_perm_b32 v83, v60, v56, s62\n"
      "v_perm_b32 v84, v53, v49, s61\n"
      "v_perm_b32 v85, v61, v57, s61\n"
      "v_perm_b32 v86, v53, v49, s62\n"
      "v_perm_b32 v87, v61, v57, s62\n"
      "v_perm_b32 v88, v54, v50, s61\n"
      "v_perm_b32 v89, v62, v58, s61\n"
      "v_perm_b32 v90, v54, v50, s62\n"
      "v_perm_b32 v91, v62, v58, s62\n"
      "v_perm_b32 v92, v55, v51, s61\n"
      "v_perm_b32 v93, v63, v59, s61\n"
      "v_perm_b32 v94, v55, v51, s62\n"
      "v_perm_b32 v95, v63, v59, s62\n"
      "s_bitcmp1_b32 s52, 0\n"
      "s_cselect_b32 s46, 0, s49\n"
      "buffer_load_dwordx4 v[48:51], v19, s[44:47], s52 offen sc0 sc1 nt\n"
      "s_bitcmp1_b32 s53, 0\n"
      "s_cselect_b32 s46, 0, s49\n"
      "buffer_load_dwordx4 v[52:55], v19, s[44:47], s53 offen sc0 sc1 nt\n"
      "s_bitcmp1_b32 s54, 0\n"
      "s_cselect_b32 s46, 0, s49\n"
      "buffer_load_dwordx4 v[56:59], v19, s[44:47], s54 offen sc0 sc1 nt\n"
      "s_bitcmp1_b32 s55, 0\n"
      "s_cselect_b32 s46, 0, s49\n"
      "buffer_load_dwordx4 v[60:63], v19, s[44:47], s55 offen sc0 sc1 nt\n"
      "v_mfma_f32_16x16x16_bf16 v[96:99], v[66:67], v[80:81], v[96:99]\n"
      "v_mfma_f32_16x16x16_bf16 v[100:103], v[66:67], v[82:83], v[100:103]\n"
      "v_mfma_f32_16x16x16_bf16 v[104:107], v[66:67], v[84:85], v[104:107]\n"
      "v_mfma_f32_16x16x16_bf16 v[108:111], v[66:67], v[86:87], v[108:111]\n"
      "v_mfma_f32_16x16x16_bf16 v[112:115], v[66:67], v[88:89], v[112:115]\n"
      "v_mfma_f32_16x16x16_bf16 v[116:119], v[66:67], v[90:91], v[116:119]\n"
      "v_mfma_f32_16x16x16_bf16 v[120:123], v[66:67], v[92:93], v[120:123]\n"
      "v_mfma_f32_16x16x16_bf16 v[124:127], v[66:67], v[94:95], v[124:127]\n"
      "s_cmp_lt_u32 s48, s70\n"
      "s_cbranch_scc1 4b\n"
      "s_cmp_eq_u32 s70, s60\n"
      "s_cbranch_scc1 6f\n"
      "s_lshl_b32 s51, s48, 2\n"
      "s_add_u32 s51, s51, 4\n"
      "v_readlane_b32 s52, v27, s51\n"
      "s_add_u32 s51, s51, 1\n"
      "v_readlane_b32 s53, v27, s51\n"
      "s_add_u32 s51, s51, 1\n"
      "v_readlane_b32 s54, v27, s51\n"
      "s_add_u32 s51, s51, 1\n"
      "v_readlane_b32 s55, v27, s51\n"
      "s_add_u32 s51, s51, 1\n"
      "s_mov_b32 s42, s50\n"
      "s_mov_b32 s46, s50\n"
      "s_waitcnt vmcnt(4)\n"
      "v_mfma_f32_16x16x32_bf16 v[64:67], v[32:35], v[20:23], 0\n"
      "v_mfma_f32_16x16x32_bf16 v[68:71], v[36:39], v[20:23], 0\n"
      "v_mfma_f32_16x16x32_bf16 v[72:75], v[40:43], v[20:23], 0\n"
      "v_mfma_f32_16x16x32_bf16 v[76:79], v[44:47], v[20:23], 0\n"
      "buffer_load_dwordx4 v[32:35], v18, s[40:43], s52 offen sc0 sc1 nt\n"
      "buffer_load_dwordx4 v[36:39], v18, s[40:43], s53 offen sc0 sc1 nt\n"
      "buffer_load_dwordx4 v[40:43], v18, s[40:43], s54 offen sc0 sc1 nt\n"
      "buffer_load_dwordx4 v[44:47], v18, s[40:43], s55 offen sc0 sc1 nt\n"
      "buffer_load_dwordx4 v[128:131], v19, s[44:47], s52 offen sc0 sc1 nt\n"
      "buffer_load_dwordx4 v[132:135], v19, s[44:47], s53 offen sc0 sc1 nt\n"
      "buffer_load_dwordx4 v[136:139], v19, s[44:47], s54 offen sc0 sc1 nt\n"
      "buffer_load_dwordx4 v[140:143], v19, s[44:47], s55 offen sc0 sc1 nt\n"
      "v_mov_b32_dpp v64, v64 quad_perm:[0,0,0,0] row_mask:0xf bank_mask:0xf\n"
      "v_mov_b32_dpp v68, v68 quad_perm:[0,0,0,0] row_mask:0xf bank_mask:0xf\n"
      "v_mov_b32_dpp v72, v72 quad_perm:[0,0,0,0] row_mask:0xf bank_mask:0xf\n"
      "v_mov_b32_dpp v76, v76 quad_perm:[0,0,0,0] row_mask:0xf bank_mask:0xf\n"
      "v_add_f32_dpp v64, v65, v64 quad_perm:[1,1,1,1] row_mask:0xf bank_mask:0xf\n"
      "v_add_f32_dpp v68, v69, v68 quad_perm:[1,1,1,1] row_mask:0xf bank_mask:0xf\n"
      "v_add_f32_dpp v72, v73, v72 quad_perm:[1,1,1,1] row_mask:0xf bank_mask:0xf\n"
      "v_add_f32_dpp v76, v77, v76 quad_perm:[1,1,1,1] row_mask:0xf bank_mask:0xf\n"
      "v_add_f32_dpp v64, v66, v64 quad_perm:[2,2,2,2] row_mask:0xf bank_mask:0xf\n"
      "v_add_f32_dpp v68, v70, v68 quad_perm:[2,2,2,2] row_mask:0xf bank_mask:0xf\n"
      "v_add_f32_dpp v72, v74, v72 quad_perm:[2,2,2,2] row_mask:0xf bank_mask:0xf\n"
      "v_add_f32_dpp v76, v78, v76 quad_perm:[2,2,2,2] row_mask:0xf bank_mask:0xf\n"
      "v_add_f32_dpp v64, v67, v64 quad_perm:[3,3,3,3] row_mask:0xf bank_mask:0xf\n"
      "v_add_f32_dpp v68, v71, v68 quad_perm:[3,3,3,3] row_mask:0xf bank_mask:0xf\n"
      "v_add_f32_dpp v72, v75, v72 quad_perm:[3,3,3,3] row_mask:0xf bank_mask:0xf\n"
      "v_add_f32_dpp v76, v79, v76 quad_perm:[3,3,3,3] row_mask:0xf bank_mask:0xf\n"
      "v_max3_f32 v65, v64, v68, v72\n"
      "v_max_f32 v65, v65, v76\n"
      "v_cmp_gt_f32 vcc, v65, v28\n"
      "s_cmp_lg_u64 vcc, 0\n"
      "s_cbranch_scc1 14f\n"
      "15:\n"
      "v_fma_f32 v65, v64, s63, v29\n"
      "v_fma_f32 v69, v68, s63, v29\n"
      "v_fma_f32 v73, v72, s63, v29\n"
      "v_fma_f32 v77, v76, s63, v29\n"
      "v_exp_f32 v65, v65\n"
      "v_exp_f32 v69, v69\n"
      "v_exp_f32 v73, v73\n"
      "v_exp_f32 v77, v77\n"
      "v_cvt_pk_bf16_f32 v66, v65, v69\n"
      "v_add_f32 v64, v65, v69\n"
      "v_cvt_pk_bf16_f32 v67, v73, v77\n"
      "v_add_f32 v68, v73, v77\n"
      "v_add_f32 v30, v30, v64\n"
      "v_add_f32 v30, v30, v68\n"
      "s_waitcnt vmcnt(8)\n"
      "v_perm_b32 v80, v52, v48, s61\n"
      "v_perm_b32 v81, v60, v56, s61\n"
      "v_perm_b32 v82, v52, v48, s62\n"
      "v_perm_b32 v83, v60, v56, s62\n"
      "v_perm_b32 v84, v53, v49, s61\n"
      "v_perm_b32 v85, v61, v57, s61\n"
      "v_perm_b32 v86, v53, v49, s62\n"
      "v_perm_b32 v87, v61, v57, s62\n"
      "v_perm_b32 v88, v54, v50, s61\n"
      "v_perm_b32 v89, v62, v58, s61\n"
      "v_perm_b32 v90, v54, v50, s62\n"
      "v_perm_b32 v91, v62, v58, s62\n"
      "v_perm_b32 v92, v55, v51, s61\n"
      "v_perm_b32 v93, v63, v59, s61\n"
      "v_perm_b32 v94, v55, v51, s62\n"
      "v_perm_b32 v95, v63, v59, s62\n"
      "s_nop 1\n"
      "v_mfma_f32_16x16x16_bf16 v[96:99], v[66:67], v[80:81], v[96:99]\n"
      "v_mfma_f32_16x16x16_bf16 v[100:103], v[66:67], v[82:83], v[100:103]\n"
      "v_mfma_f32_16x16x16_bf16 v[104:107], v[66:67], v[84:85], v[104:107]\n"
      "v_mfma_f32_16x16x16_bf16 v[108:111], v[66:67], v[86:87], v[108:111]\n"
      "v_mfma_f32_16x16x16_bf16 v[112:115], v[66:67], v[88:89], v[112:115]\n"
      "v_mfma_f32_16x16x16_bf16 v[116:119], v[66:67], v[90:91], v[116:119]\n"
      "v_mfma_f32_16x16x16_bf16 v[120:123], v[66:67], v[92:93], v[120:123]\n"
      "v_mfma_f32_16x16x16_bf16 v[124:127], v[66:67], v[94:95], v[124:127]\n"
      "s_waitcnt vmcnt(4)\n"
      "v_mfma_f32_16x16x32_bf16 v[64:67], v[32:35], v[20:23], 0\n"
      "v_mfma_f32_16x16x32_bf16 v[68:71], v[36:39], v[20:23], 0\n"
      "v_mfma_f32_16x16x32_bf16 v[72:75], v[40:43], v[20:23], 0\n"
      "v_mfma_f32_16x16x32_bf16 v[76:79], v[44:47], v[20:23], 0\n"
      "s_nop 7\n"
      "v_mov_b32_dpp v64, v64 quad_perm:[0,0,0,0] row_mask:0xf bank_mask:0xf\n"
      "v_mov_b32_dpp v68, v68 quad_perm:[0,0,0,0] row_mask:0xf bank_mask:0xf\n"
      "v_mov_b32_dpp v72, v72 quad_perm:[0,0,0,0] row_mask:0xf bank_mask:0xf\n"
      "v_mov_b32_dpp v76, v76 quad_perm:[0,0,0,0] row_mask:0xf bank_mask:0xf\n"
      "v_add_f32_dpp v64, v65, v64 quad_perm:[1,1,1,1] row_mask:0xf bank_mask:0xf\n"
      "v_add_f32_dpp v68, v69, v68 quad_perm:[1,1,1,1] row_mask:0xf bank_mask:0xf\n"
      "v_add_f32_dpp v72, v73, v72 quad_perm:[1,1,1,1] row_mask:0xf bank_mask:0xf\n"
      "v_add_f32_dpp v76, v77, v76 quad_perm:[1,1,1,1] row_mask:0xf bank_mask:0xf\n"
      "v_add_f32_dpp v64, v66, v64 quad_perm:[2,2,2,2] row_mask:0xf bank_mask:0xf\n"
      "v_add_f32_dpp v68, v70, v68 quad_perm:[2,2,2,2] row_mask:0xf bank_mask:0xf\n"
      "v_add_f32_dpp v72, v74, v72 quad_perm:[2,2,2,2] row_mask:0xf bank_mask:0xf\n"
      "v_add_f32_dpp v76, v78, v76 quad_perm:[2,2,2,2] row_mask:0xf bank_mask:0xf\n"
      "v_add_f32_dpp v64, v67, v64 quad_perm:[3,3,3,3] row_mask:0xf bank_mask:0xf\n"
      "v_add_f32_dpp v68, v71, v68 quad_perm:[3,3,3,3] row_mask:0xf bank_mask:0xf\n"
      "v_add_f32_dpp v72, v75, v72 quad_perm:[3,3,3,3] row_mask:0xf bank_mask:0xf\n"
      "v_add_f32_dpp v76, v79, v76 quad_perm:[3,3,3,3] row_mask:0xf bank_mask:0xf\n"
      "v_max3_f32 v65, v64, v68, v72\n"
      "v_max_f32 v65, v65, v76\n"
      "v_cmp_gt_f32 vcc, v65, v28\n"
      "s_cmp_lg_u64 vcc, 0\n"
      "s_cbranch_scc1 16f\n"
      "17:\n"
      "v_fma_f32 v65, v64, s63, v29\n"
      "v_fma_f32 v69, v68, s63, v29\n"
      "v_fma_f32 v73, v72, s63, v29\n"
      "v_fma_f32 v77, v76, s63, v29\n"
      "v_exp_f32 v65, v65\n"
      "v_exp_f32 v69, v69\n"
      "v_exp_f32 v73, v73\n"
      "v_exp_f32 v77, v77\n"
      "v_cvt_pk_bf16_f32 v66, v65, v69\n"
      "v_add_f32 v64, v65, v69\n"
      "v_cvt_pk_bf16_f32 v67, v73, v77\n"
      "v_add_f32 v68, v73, v77\n"
      "v_add_f32 v30, v30, v64\n"
      "v_add_f32 v30, v30, v68\n"
      "s_waitcnt vmcnt(0)\n"
      "v_perm_b32 v80, v132, v128, s61\n"
      "v_perm_b32 v81, v140, v136, s61\n"
      "v_perm_b32 v82, v132, v128, s62\n"
      "v_perm_b32 v83, v140, v136, s62\n"
      "v_perm_b32 v84, v133, v129, s61\n"
      "v_perm_b32 v85, v141, v137, s61\n"
      "v_perm_b32 v86, v133, v129, s62\n"
      "v_perm_b32 v87, v141, v137, s62\n"
      "v_perm_b32 v88, v134, v130, s61\n"
      "v_perm_b32 v89, v142, v138, s61\n"
      "v_perm_b32 v90, v134, v130, s62\n"
      "v_perm_b32 v91, v142, v138, s62\n"
      "v_perm_b32 v92, v135, v131, s61\n"
      "v_perm_b32 v93, v143, v139, s61\n"
      "v_perm_b32 v94, v135, v131, s62\n"
      "v_perm_b32 v95, v143, v139, s62\n"
      "s_nop 1\n"
      "v_mfma_f32_16x16x16_bf16 v[96:99], v[66:67], v[80:81], v[96:99]\n"
      "v_mfma_f32_16x16x16_bf16 v[100:103], v[66:67], v[82:83], v[100:103]\n"
      "v_mfma_f32_16x16x16_bf16 v[104:107], v[66:67], v[84:85], v[104:107]\n"
      "v_mfma_f32_16x16x16_bf16 v[108:111], v[66:67], v[86:87], v[108:111]\n"
      "v_mfma_f32_16x16x16_bf16 v[112:115], v[66:67], v[88:89], v[112:115]\n"
      "v_mfma_f32_16x16x16_bf16 v[116:119], v[66:67], v[90:91], v[116:119]\n"
      "v_mfma_f32_16x16x16_bf16 v[120:123], v[66:67], v[92:93], v[120:123]\n"
      "v_mfma_f32_16x16x16_bf16 v[124:127], v[66:67], v[94:95], v[124:127]\n"
      "6:\n"
      "s_waitcnt vmcnt(0)\n"
      "v_max_f32_e64 v28, -v29, s78\n"
      "v_mov_b32 v60, v31\n"
      "v_add_u32 v31, 1, v31\n"
      "ds_write2_b32 v178, v30, v28 offset0:0 offset1:4\n"
      "v_and_b32 v192, 0xffffff, v31\n"
      "s_nop 4\n"
      "ds_write2st64_b32 v176, v96, v100 offset0:0 offset1:1\n"
      "ds_write2st64_b32 v176, v104, v108 offset0:2 offset1:3\n"
      "ds_write2st64_b32 v176, v112, v116 offset0:4 offset1:5\n"
      "ds_write2st64_b32 v176, v120, v124 offset0:6 offset1:7\n"
      "s_mov_b64 exec, 1\n"
      "ds_wrxchg_rtn_b32 v32, v181, v191\n"
      "ds_add_rtn_u32 v15, v11, v9\n"
      "s_mov_b64 exec, -1\n"
      "ds_read_b32 v33, v179 offset:16\n"
      "ds_read_b32 v34, v180 offset:16\n"
      "ds_read_b128 v[36:39], v179\n"
      "ds_read_b128 v[40:43], v180\n"
      "ds_read2st64_b32 v[44:45], v177 offset0:0 offset1:1\n"
      "ds_read2st64_b32 v[46:47], v177 offset0:2 offset1:3\n"
      "ds_read2st64_b32 v[48:49], v177 offset0:4 offset1:5\n"
      "ds_read2st64_b32 v[50:51], v177 offset0:6 offset1:7\n"
      "s_waitcnt lgkmcnt(8)\n"
      "v_readfirstlane_b32 s48, v32\n"
      "s_cmp_lg_u32 s48, 0\n"
      "s_cselect_b64 s[82:83], -1, 0\n"
      "s_cbranch_scc0 20f\n"
      "v_readfirstlane_b32 s36, v15\n"
      "s_cmp_eq_u32 s36, 7\n"
      "s_cselect_b64 exec, 0x1, 0\n"
      "global_atomic_add v14, v10, v9, s[16:17] sc0 sc1\n"
      "s_mov_b64 exec, -1\n"
      "s_waitcnt lgkmcnt(4)\n"
      "v_max_f32 v35, v33, v34\n"
      "v_add_f32 v36, v36, v37\n"
      "v_add_f32 v40, v40, v41\n"
      "v_sub_f32 v33, v33, v35\n"
      "v_sub_f32 v34, v34, v35\n"
      "v_add_f32 v38, v38, v39\n"
      "v_add_f32 v42, v42, v43\n"
      "v_exp_f32 v33, v33\n"
      "v_exp_f32 v34, v34\n"
      "v_add_f32 v36, v36, v38\n"
      "v_add_f32 v40, v40, v42\n"
      "v_mul_f32 v36, v36, v33\n"
      "v_mul_f32 v40, v40, v34\n"
      "v_add_f32 v36, v36, v40\n"
      "v_rcp_f32 v37, v36\n"
      "v_log_f32 v38, v36\n"
      "v_min_f32_e64 v37, v37, -s78\n"
      "v_add_f32 v38, v38, v35\n"
      "v_mul_f32 v40, v34, v37\n"
      "v_mul_f32 v33, v33, v37\n"
      "v_max_f32 v38, s78, v38\n"
      "s_waitcnt lgkmcnt(0)\n"
      "v_pk_mul_f32 v[44:45], v[44:45], v[40:41] op_sel_hi:[1,0]\n"
      "v_pk_mul_f32 v[46:47], v[46:47], v[40:41] op_sel_hi:[1,0]\n"
      "v_pk_mul_f32 v[48:49], v[48:49], v[40:41] op_sel_hi:[1,0]\n"
      "v_pk_mul_f32 v[50:51], v[50:51], v[40:41] op_sel_hi:[1,0]\n"
      "v_lshrrev_b32 v38, v184, v38\n"
      "v_mul_f32 v52, v33, v96\n"
      "v_mul_f32 v53, v33, v100\n"
      "v_mul_f32 v54, v33, v104\n"
      "v_mul_f32 v55, v33, v108\n"
      "v_mul_f32 v56, v33, v112\n"
      "v_mul_f32 v57, v33, v116\n"
      "v_mul_f32 v58, v33, v120\n"
      "v_mul_f32 v59, v33, v124\n"
      "v_pk_add_f32 v[52:53], v[52:53], v[44:45]\n"
      "v_pk_add_f32 v[54:55], v[54:55], v[46:47]\n"
      "v_pk_add_f32 v[56:57], v[56:57], v[48:49]\n"
      "v_pk_add_f32 v[58:59], v[58:59], v[50:51]\n"
      "v_perm_b32 v52, v52, v31, s79\n"
      "v_perm_b32 v53, v53, v31, s76\n"
      "v_perm_b32 v54, v54, v31, s77\n"
      "v_perm_b32 v55, v55, v38, s79\n"
      "global_store_dwordx4 v182, v[52:55], s[16:17] sc1\n"
      "v_perm_b32 v56, v56, v31, s79\n"
      "v_perm_b32 v57, v57, v31, s76\n"
      "v_perm_b32 v58, v58, v31, s77\n"
      "v_perm_b32 v59, v59, v38, s79\n"
      "global_store_dwordx4 v183, v[56:59], s[16:17] sc1\n"
      "s_branch 21f\n"
      "20:\n"
      "v_readfirstlane_b32 s36, v15\n"
      "s_cmp_eq_u32 s36, 0\n"
      "s_cbranch_scc0 28f\n"
      "s_mov_b64 exec, 0\n"
      "s_branch 23f\n"
      "21:\n"
      "s_cmp_eq_u32 s36, 7\n"
      "s_cbranch_scc0 48f\n"
      "s_waitcnt vmcnt(2)\n"
      "v_readfirstlane_b32 s49, v14\n"
      "s_sub_u32 s49, s49, s38\n"
      "s_sub_u32 s49, 127, s49\n"
      "s_min_u32 s49, s49, 128\n"
      "s_mul_i32 s48, s49, 143\n"
      "s_mul_i32 s49, s49, 88\n"
      "s_add_u32 s49, s49, 1100\n"
      "s_min_u32 s48, s48, s49\n"
      "s_mul_i32 s48, s48, s37\n"
      "s_lshr_b32 s48, s48, 11\n"
      "s_cbranch_scc0 48f\n"
      "49:\n"
      "s_sleep 1\n"
      "s_sub_u32 s48, s48, 1\n"
      "s_cmp_lg_u32 s48, 0\n"
      "s_cbranch_scc1 49b\n"
      "48:\n"
      "22:\n"
      "s_barrier\n"
      "23:\n"
      "s_mov_b32 s48, 0\n"
      "v_perm_b32 v64, v61, v60, s80\n"
      "v_perm_b32 v64, v62, v64, s81\n"
      "v_cmp_ne_u32 vcc, v64, v192\n"
      "s_wqm_b64 vcc, vcc\n"
      "s_and_b64 exec, exec, vcc\n"
      "s_cbranch_scc0 25f\n"
      "24:\n"
      "global_load_dwordx4 v[60:63], v185, s[16:17] sc1\n"
      "s_add_u32 s48, s48, 1\n"
      "s_waitcnt vmcnt(0)\n"
      "v_perm_b32 v64, v61, v60, s80\n"
      "v_perm_b32 v64, v62, v64, s81\n"
      "v_cmp_ne_u32 vcc, v64, v192\n"
      "s_wqm_b64 vcc, vcc\n"
      "s_and_b64 exec, exec, vcc\n"
      "s_cbranch_scc0 25f\n"
      "s_bcnt1_i32_b64 s49, exec\n"
      "s_cmp_le_u32 s49, 8\n"
      "s_cbranch_scc1 40f\n"
      "s_sleep 2\n"
      "s_cmp_lt_u32 s48, 0x20000\n"
      "s_cbranch_scc1 24b\n"
      "s_branch 25f\n"
      "40:\n"
      "global_load_dwordx4 v[72:75], v185, s[16:17] sc1\n"
      "s_sleep 4\n"
      "global_load_dwordx4 v[76:79], v185, s[16:17] sc1\n"
      "s_sleep 4\n"
      "41:\n"
      "global_load_dwordx4 v[80:83], v185, s[16:17] sc1\n"
      "s_waitcnt vmcnt(2)\n"
      "v_mov_b64 v[60:61], v[72:73]\n"
      "v_mov_b64 v[62:63], v[74:75]\n"
      "v_perm_b32 v64, v61, v60, s80\n"
      "v_perm_b32 v64, v62, v64, s81\n"
      "v_cmp_ne_u32 vcc, v64, v192\n"
      "s_wqm_b64 vcc, vcc\n"
      "s_and_b64 exec, exec, vcc\n"
      "s_cbranch_scc0 25f\n"
      "global_load_dwordx4 v[72:75], v185, s[16:17] sc1\n"
      "s_waitcnt vmcnt(2)\n"
      "v_mov_b64 v[60:61], v[76:77]\n"
      "v_mov_b64 v[62:63], v[78:79]\n"
      "v_perm_b32 v64, v61, v60, s80\n"
      "v_perm_b32 v64, v62, v64, s81\n"
      "v_cmp_ne_u32 vcc, v64, v192\n"
      "s_wqm_b64 vcc, vcc\n"
      "s_and_b64 exec, exec, vcc\n"
      "s_cbranch_scc0 25f\n"
      "global_load_dwordx4 v[76:79], v185, s[16:17] sc1\n"
      "s_waitcnt vmcnt(2)\n"
      "v_mov_b64 v[60:61], v[80:81]\n"
      "v_mov_b64 v[62:63], v[82:83]\n"
      "v_perm_b32 v64, v61, v60, s80\n"
      "v_perm_b32 v64, v62, v64, s81\n"
      "v_cmp_ne_u32 vcc, v64, v192\n"
      "s_wqm_b64 vcc, vcc\n"
      "s_and_b64 exec, exec, vcc\n"
      "s_cbranch_scc0 25f\n"
      "s_add_u32 s48, s48, 1\n"
      "s_cmp_lt_u32 s48, 0x20000\n"
      "s_cbranch_scc1 41b\n"
      "25:\n"
      "s_mov_b64 exec, s[82:83]\n"
      "v_lshlrev_b32_sdwa v64, v184, v63 dst_sel:DWORD dst_unused:UNUSED_PAD src0_sel:DWORD src1_sel:BYTE_0\n"
      "v_perm_b32 v60, v60, v191, s79\n"
      "v_perm_b32 v61, v61, v191, s79\n"
      "v_perm_b32 v62, v62, v191, s79\n"
      "v_perm_b32 v63, v63, v191, s79\n"
      "v_or_b32_dpp v64, v64, v64 quad_perm:[1,0,3,2] row_mask:0xf bank_mask:0xf\n"
      "s_nop 1\n"
      "v_or_b32_dpp v64, v64, v64 quad_perm:[2,3,0,1] row_mask:0xf bank_mask:0xf\n"
      "s_nop 1\n"
      "v_max_f32_dpp v66, v64, v64 row_ror:4 row_mask:0xf bank_mask:0xf\n"
      "s_nop 1\n"
      "v_max_f32_dpp v66, v66, v66 row_ror:8 row_mask:0xf bank_mask:0xf\n"
      "v_mov_b32 v65, v66\n"
      "s_nop 1\n"
      "v_permlane16_swap_b32 v66, v65\n"
      "v_max_f32 v66, v66, v65\n"
      "v_mov_b32 v65, v66\n"
      "s_nop 1\n"
      "v_permlane32_swap_b32 v66, v65\n"
      "v_max_f32 v66, v66, v65\n"
      "v_sub_f32 v64, v64, v66\n"
      "v_exp_f32 v64, v64\n"
      "s_nop 0\n"
      "v_pk_mul_f32 v[60:61], v[60:61], v[64:65] op_sel_hi:[1,0]\n"
      "v_pk_mul_f32 v[62:63], v[62:63], v[64:65] op_sel_hi:[1,0]\n"
      "v_add_f32_dpp v67, v64, v64 row_ror:4 row_mask:0xf bank_mask:0xf\n"
      "s_nop 0\n"
      "v_permlane32_swap_b32 v60, v62\n"
      "v_permlane32_swap_b32 v61, v63\n"
      "v_add_f32_dpp v67, v67, v67 row_ror:8 row_mask:0xf bank_mask:0xf\n"
      "v_pk_add_f32 v[60:61], v[60:61], v[62:63]\n"
      "v_mov_b32 v65, v67\n"
      "s_nop 0\n"
      "v_permlane16_swap_b32 v60, v61\n"
      "v_permlane16_swap_b32 v67, v65\n"
      "v_add_f32 v60, v60, v61\n"
      "v_add_f32 v67, v67, v65\n"
      "v_mov_b32 v65, v67\n"
      "v_add_f32_dpp v60, v60, v60 row_ror:4 row_mask:0xf bank_mask:0xf\n"
      "s_nop 0\n"
      "v_permlane32_swap_b32 v67, v65\n"
      "v_add_f32_dpp v60, v60, v60 row_ror:8 row_mask:0xf bank_mask:0xf\n"
      "v_add_f32 v67, v67, v65\n"
      "ds_write_b32 v187, v60\n"
      "s_and_b64 exec, s[82:83], 1\n"
      "ds_write_b64 v187, v[66:67] offset:64\n"
      "ds_add_rtn_u32 v15, v11, v9 offset:4\n"
      "s_mov_b64 exec, s[82:83]\n"
      "ds_read_b64 v[32:33], v188 offset:64\n"
      "ds_read_b64 v[34:35], v189\n"
      "s_waitcnt lgkmcnt(2)\n"
      "v_readfirstlane_b32 s48, v15\n"
      "s_cbranch_execz 26f\n"
      "s_cmp_lg_u32 s48, 7\n"
      "s_cbranch_scc1 29f\n"
      "26:\n"
      "s_waitcnt lgkmcnt(1)\n"
      "v_max_f32_dpp v36, v32, v32 row_ror:8 row_mask:0xf bank_mask:0xf\n"
      "v_mov_b32 v37, v36\n"
      "s_nop 1\n"
      "v_permlane16_swap_b32 v36, v37\n"
      "v_max_f32 v36, v36, v37\n"
      "v_mov_b32 v37, v36\n"
      "s_nop 1\n"
      "v_permlane32_swap_b32 v36, v37\n"
      "v_max_f32 v36, v36, v37\n"
      "v_sub_f32 v36, v32, v36\n"
      "v_exp_f32 v36, v36\n"
      "s_waitcnt lgkmcnt(0)\n"
      "v_mul_f32 v33, v33, v36\n"
      "v_pk_mul_f32 v[34:35], v[34:35], v[36:37] op_sel_hi:[1,0]\n"
      "s_nop 0\n"
      "v_add_f32_dpp v33, v33, v33 row_ror:8 row_mask:0xf bank_mask:0xf\n"
      "v_add_f32_dpp v34, v34, v34 row_ror:8 row_mask:0xf bank_mask:0xf\n"
      "v_add_f32_dpp v35, v35, v35 row_ror:8 row_mask:0xf bank_mask:0xf\n"
      "v_mov_b32 v37, v33\n"
      "v_mov_b32 v38, v34\n"
      "v_mov_b32 v39, v35\n"
      "v_permlane16_swap_b32 v33, v37\n"
      "v_permlane16_swap_b32 v34, v38\n"
      "v_permlane16_swap_b32 v35, v39\n"
      "v_add_f32 v33, v33, v37\n"
      "v_add_f32 v34, v34, v38\n"
      "v_add_f32 v35, v35, v39\n"
      "v_mov_b32 v37, v33\n"
      "v_mov_b32 v38, v34\n"
      "v_mov_b32 v39, v35\n"
      "v_permlane32_swap_b32 v33, v37\n"
      "v_permlane32_swap_b32 v34, v38\n"
      "v_permlane32_swap_b32 v35, v39\n"
      "v_add_f32 v33, v33, v37\n"
      "v_add_f32 v34, v34, v38\n"
      "v_add_f32 v35, v35, v39\n"
      "v_rcp_f32 v32, v33\n"
      "s_and_b64 exec, s[82:83], 0xff\n"
      "v_pk_mul_f32 v[34:35], v[34:35], v[32:33] op_sel_hi:[1,0]\n"
      "v_cvt_pk_bf16_f32 v34, v34, v35\n"
      "global_store_dword v190, v34, s[14:15] sc0 sc1\n"
      "s_cmp_eq_u32 s18, 0\n"
      "s_cselect_b64 s[56:57], 1, 0\n"
      "s_and_b64 exec, s[82:83], s[56:57]\n"
      "v_mov_b32 v32, s26\n"
      "global_store_dword v32, v31, s[16:17] sc1\n"
      "s_cmp_lg_u64 s[82:83], 0\n"
      "s_cbranch_scc0 28f\n"
      "29:\n"
      "s_endpgm\n"
      "28:\n"
      "s_mov_b64 s[82:83], -1\n"
      "s_mov_b64 exec, -1\n"
      "s_waitcnt vmcnt(0) lgkmcnt(0)\n"
      "v_add_u32 v60, -1, v31\n"
      "s_branch 22b\n"
      "10:\n"
      "v_mov_b32 v69, 0xff800000\n"
      "v_cmp_gt_u32 vcc, s56, v26\n"
      "s_nop 1\n"
      "v_cndmask_b32 v64, v69, v64, vcc\n"
      "v_cmp_gt_u32 vcc, s57, v26\n"
      "s_nop 1\n"
      "v_cndmask_b32 v68, v69, v68, vcc\n"
      "v_cmp_gt_u32 vcc, s58, v26\n"
      "s_nop 1\n"
      "v_cndmask_b32 v72, v69, v72, vcc\n"
      "v_cmp_gt_u32 vcc, s59, v26\n"
      "s_nop 1\n"
      "v_cndmask_b32 v76, v69, v76, vcc\n"
      "s_branch 11b\n"
      "12:\n"
      "v_mov_b32 v69, v65\n"
      "s_nop 1\n"
      "v_permlane16_swap_b32 v65, v69\n"
      "s_nop 1\n"
      "v_max_f32 v65, v65, v69\n"
      "v_mov_b32 v69, v65\n"
      "s_nop 1\n"
      "v_permlane32_swap_b32 v65, v69\n"
      "s_nop 1\n"
      "v_max3_f32 v28, v65, v69, v28\n"
      "v_mul_f32 v65, s63, v28\n"
      "v_add_f32 v69, v65, v29\n"
      "v_exp_f32_e64 v69, -v69\n"
      "v_sub_f32 v29, 0, v65\n"
      "v_mul_f32 v30, v30, v69\n"
      "s_nop 1\n"
      "v_mov_b32_dpp v73, v69 row_newbcast:0 row_mask:0x1 bank_mask:0xf\n"
      "s_nop 1\n"
      "v_mov_b32_dpp v73, v69 row_newbcast:4 row_mask:0x2 bank_mask:0xf\n"
      "s_nop 1\n"
      "v_mov_b32_dpp v73, v69 row_newbcast:8 row_mask:0x4 bank_mask:0xf\n"
      "s_nop 1\n"
      "v_mov_b32_dpp v73, v69 row_newbcast:12 row_mask:0x8 bank_mask:0xf\n"
      "s_nop 1\n"
      "v_mul_f32 v96, v73, v96\n"
      "v_mul_f32 v100, v73, v100\n"
      "v_mul_f32 v104, v73, v104\n"
      "v_mul_f32 v108, v73, v108\n"
      "v_mul_f32 v112, v73, v112\n"
      "v_mul_f32 v116, v73, v116\n"
      "v_mul_f32 v120, v73, v120\n"
      "v_mul_f32 v124, v73, v124\n"
      "s_branch 13b\n"
      "14:\n"
      "v_mov_b32 v69, v65\n"
      "s_nop 1\n"
      "v_permlane16_swap_b32 v65, v69\n"
      "s_nop 1\n"
      "v_max_f32 v65, v65, v69\n"
      "v_mov_b32 v69, v65\n"
      "s_nop 1\n"
      "v_permlane32_swap_b32 v65, v69\n"
      "s_nop 1\n"
      "v_max3_f32 v28, v65, v69, v28\n"
      "v_mul_f32 v65, s63, v28\n"
      "v_add_f32 v69, v65, v29\n"
      "v_exp_f32_e64 v69, -v69\n"
      "v_sub_f32 v29, 0, v65\n"
      "v_mul_f32 v30, v30, v69\n"
      "s_nop 1\n"
      "v_mov_b32_dpp v73, v69 row_newbcast:0 row_mask:0x1 bank_mask:0xf\n"
      "s_nop 1\n"
      "v_mov_b32_dpp v73, v69 row_newbcast:4 row_mask:0x2 bank_mask:0xf\n"
      "s_nop 1\n"
      "v_mov_b32_dpp v73, v69 row_newbcast:8 row_mask:0x4 bank_mask:0xf\n"
      "s_nop 1\n"
      "v_mov_b32_dpp v73, v69 row_newbcast:12 row_mask:0x8 bank_mask:0xf\n"
      "s_nop 1\n"
      "v_mul_f32 v96, v73, v96\n"
      "v_mul_f32 v100, v73, v100\n"
      "v_mul_f32 v104, v73, v104\n"
      "v_mul_f32 v108, v73, v108\n"
      "v_mul_f32 v112, v73, v112\n"
      "v_mul_f32 v116, v73, v116\n"
      "v_mul_f32 v120, v73, v120\n"
      "v_mul_f32 v124, v73, v124\n"
      "s_branch 15b\n"
      "16:\n"
      "v_mov_b32 v69, v65\n"
      "s_nop 1\n"
      "v_permlane16_swap_b32 v65, v69\n"
      "s_nop 1\n"
      "v_max_f32 v65, v65, v69\n"
      "v_mov_b32 v69, v65\n"
      "s_nop 1\n"
      "v_permlane32_swap_b32 v65, v69\n"
      "s_nop 1\n"
      "v_max3_f32 v28, v65, v69, v28\n"
      "v_mul_f32 v65, s63, v28\n"
      "v_add_f32 v69, v65, v29\n"
      "v_exp_f32_e64 v69, -v69\n"
      "v_sub_f32 v29, 0, v65\n"
      "v_mul_f32 v30, v30, v69\n"
      "s_nop 1\n"
      "v_mov_b32_dpp v73, v69 row_newbcast:0 row_mask:0x1 bank_mask:0xf\n"
      "s_nop 1\n"
      "v_mov_b32_dpp v73, v69 row_newbcast:4 row_mask:0x2 bank_mask:0xf\n"
      "s_nop 1\n"
      "v_mov_b32_dpp v73, v69 row_newbcast:8 row_mask:0x4 bank_mask:0xf\n"
      "s_nop 1\n"
      "v_mov_b32_dpp v73, v69 row_newbcast:12 row_mask:0x8 bank_mask:0xf\n"
      "s_nop 1\n"
      "v_mul_f32 v96, v73, v96\n"
      "v_mul_f32 v100, v73, v100\n"
      "v_mul_f32 v104, v73, v104\n"
      "v_mul_f32 v108, v73, v108\n"
      "v_mul_f32 v112, v73, v112\n"
      "v_mul_f32 v116, v73, v116\n"
      "v_mul_f32 v120, v73, v120\n"
      "v_mul_f32 v124, v73, v124\n"
      "s_branch 17b\n"
      "30:\n"
      :
      : [tid] "{v0}"(threadIdx.x), [ka] "{s[0:1]}"(__builtin_amdgcn_kernarg_segment_ptr()), [bid] "{s2}"(blockIdx.x), "{s39}"((__attribute__((address_space(3))) float*)lds)
      :
        "s3", "s4", "s5", "s6", "s7", "s8", "s9", "s10", "s11", "s12", "s13", "s14", "s15", "s16", "s17", "s18", "s19", "s20", "s21", "s22", "s23",
        "s24", "s25", "s26", "s27", "s28", "s29", "s30", "s31", "s32", "s33", "s34", "s35", "s36", "s37", "s38", "s40", "s41", "s42", "s43", "s44",
        "s45", "s46", "s47", "s48", "s49", "s50", "s51", "s52", "s53", "s54", "s55", "s56", "s57", "s58", "s59", "s60", "s61", "s62", "s63", "s64",
        "s65", "s66", "s67", "s68", "s69", "s70", "s71", "s72", "s73", "s74", "s75", "s76", "s77", "s78", "s79", "s80", "s81", "s82", "s83", "vcc",
        "scc", "m0", "memory", "v1", "v2", "v3", "v4", "v5", "v6", "v7", "v8", "v9", "v10", "v11", "v12", "v13", "v14", "v15", "v16", "v17", "v18",
        "v19", "v20", "v21", "v22", "v23", "v24", "v25", "v26", "v27", "v28", "v29", "v30", "v31", "v32", "v33", "v34", "v35", "v36", "v37", "v38",
        "v39", "v40", "v41", "v42", "v43", "v44", "v45", "v46", "v47", "v48", "v49", "v50", "v51", "v52", "v53", "v54", "v55", "v56", "v57", "v58",
        "v59", "v60", "v61", "v62", "v63", "v64", "v65", "v66", "v67", "v68", "v69", "v70", "v71", "v72", "v73", "v74", "v75", "v76", "v77", "v78",
        "v79", "v80", "v81", "v82", "v83", "v84", "v85", "v86", "v87", "v88", "v89", "v90", "v91", "v92", "v93", "v94", "v95", "v96", "v97", "v98",
        "v99", "v100", "v101", "v102", "v103", "v104", "v105", "v106", "v107", "v108", "v109", "v110", "v111", "v112", "v113", "v114", "v115", "v116",
        "v117", "v118", "v119", "v120", "v121", "v122", "v123", "v124", "v125", "v126", "v127", "v128", "v129", "v130", "v131", "v132", "v133", "v134",
        "v135", "v136", "v137", "v138", "v139", "v140", "v141", "v142", "v143", "v144", "v145", "v146", "v147", "v148", "v149", "v150", "v151", "v152",
        "v153", "v154", "v155", "v156", "v157", "v158", "v159", "v160", "v161", "v162", "v163", "v164", "v165", "v166", "v167", "v168", "v169", "v170",
        "v171", "v172", "v173", "v174", "v175", "v176", "v177", "v178", "v179", "v180", "v181", "v182", "v183", "v184", "v185", "v186", "v187", "v188",
        "v189", "v190", "v191", "v192");
}
}

extern "C" void kernel_launch(void* const* d_in, const int* in_sizes, int n_in, void* d_out, int out_size, void* d_ws, size_t ws_size,
                              hipStream_t stream) {
  attn_fused<<<GRID, WAVES * 64, 0, stream>>>((const unsigned char*)d_in[0], (const unsigned char*)d_in[1], (const unsigned char*)d_in[2],
                                              (const int*)d_in[3], (const int*)d_in[4], (unsigned*)d_out, (unsigned char*)d_ws);
}
```
